```python
import torch

def _softmax(x, axis=-1):
    return torch.softmax(x, dim=axis)
def _gelu(x):
    return torch.nn.functional.gelu(x, approximate="tanh")
def _silu(x):
    return torch.nn.functional.silu(x)
def _sigmoid(x):
    return torch.sigmoid(x)
def _layernorm(x, axis=-1, eps=1e-5):
    m = x.mean(dim=axis, keepdim=True)
    v = x.var(dim=axis, keepdim=True, unbiased=False)
    return (x - m) / torch.sqrt(v + eps)
def _scatter_add(tgt, idx, src, axis=0):
    return tgt.clone().index_add_(axis, idx, src)
def _segment_sum(src, seg, n):
    out = torch.zeros((n,) + src.shape[1:], dtype=src.dtype)
    return out.index_add_(0, seg, src)
def _pad(x, pw):
    flat = tuple(v for pair in reversed(pw) for v in pair)
    return torch.nn.functional.pad(x, flat)

kernel_name = 'parallel_sum__linear_kernel_hedgehog__glu_gated_reglu__prompt'

def setup_inputs():

    _w = (lambda a: (lambda t: t.to(torch.bfloat16) if t.is_floating_point() else t)(torch.as_tensor(a)))
    def _A_setup():
        B, S, D, H = 1, 2048, 448, 16
        Dh = D // H
        return {
            "x": _w((lambda *s: torch.randn(*s, dtype=torch.bfloat16) * 0.05)(B, S, D)),
            "wqkv": _w((lambda *s: torch.randn(*s, dtype=torch.bfloat16) * 0.2)(D, 3 * D)),
            "w_phi": _w((lambda *s: torch.randn(*s, dtype=torch.bfloat16) * 0.2)(H, Dh, Dh) * (Dh ** -0.5)),
            "wo": _w((lambda *s: torch.randn(*s, dtype=torch.bfloat16) * 0.05)(D, D)),
        }
    _A_in = _A_setup()

    def _B_setup():
        B, S, D, F = 1, 2048, 448, 1792
        return {
            "x": _w((lambda *s: torch.randn(*s, dtype=torch.bfloat16) * 0.05)(B, S, D)),
            "W_gate": _w((lambda *s: torch.randn(*s, dtype=torch.bfloat16) * 0.4)(D, F) * (2.0 / D) ** 0.5),
            "W_up": _w((lambda *s: torch.randn(*s, dtype=torch.bfloat16) * 0.4)(D, F) * (2.0 / D) ** 0.5),
            "W_down": _w((lambda *s: torch.randn(*s, dtype=torch.bfloat16) * 0.4)(F, D) * (1.0 / F) ** 0.5),
        }
    _B_in = _B_setup()

    return {
        "A_x": _A_in["x"],
        "A_wqkv": _A_in["wqkv"],
        "A_w_phi": _A_in["w_phi"],
        "A_wo": _A_in["wo"],
        "B_x": _B_in["x"],
        "B_W_gate": _B_in["W_gate"],
        "B_W_up": _B_in["W_up"],
        "B_W_down": _B_in["W_down"],
    }

def reference(A_x, A_wqkv, A_w_phi, A_wo, B_x, B_W_gate, B_W_up, B_W_down):
    def _A_ref(x, wqkv, w_phi, wo):
        B, S, D = x.shape
        H = 16; Dh = D // H
        qkv = (lambda x, *d: x.permute(*d))((x @ wqkv).reshape(B, S, 3, H, Dh), 2, 0, 3, 1, 4)
        q, k, v = qkv[0], qkv[1], qkv[2]
        pq = torch.einsum("bhsd,hdf->bhsf", q, w_phi)
        pk = torch.einsum("bhsd,hdf->bhsf", k, w_phi)
        phi_q = torch.concatenate([_softmax(pq, axis=-1), _softmax(-pq, axis=-1)], axis=-1)
        phi_k = torch.concatenate([_softmax(pk, axis=-1), _softmax(-pk, axis=-1)], axis=-1)
        kv = torch.einsum("bhsf,bhsd->bhfd", phi_k, v)
        z  = torch.einsum("bhsf->bhf", phi_k)
        num = torch.einsum("bhqf,bhfd->bhqd", phi_q, kv)
        den = torch.einsum("bhqf,bhf->bhq", phi_q, z)[..., None] + 1e-6
        o = num / den
        return (lambda x, *d: x.permute(*d))(o, 0, 2, 1, 3).reshape(B, S, D) @ wo

    def _B_ref(x, W_gate, W_up, W_down):
        gate = torch.einsum("bsd,df->bsf", x, W_gate)
        up = torch.einsum("bsd,df->bsf", x, W_up)
        gate = gate * (lambda x, dt: x.to(dt))(gate > 0, gate.dtype)
        h = gate * up
        return torch.einsum("bsf,fd->bsd", h, W_down)

    x = A_x
    xn = _layernorm(x, axis=-1)
    return x + _A_ref(xn, A_wqkv, A_w_phi, A_wo) + _B_ref(xn, B_W_gate, B_W_up, B_W_down)
```

```cpp
#include <hip/hip_runtime.h>
#include <hip/hip_bf16.h>
typedef unsigned short u16;
__device__ __forceinline__ float bf2f(u16 v){ return __uint_as_float(((unsigned)v)<<16); }
__device__ __forceinline__ u16 f2bf(float f){ __hip_bfloat16 b = __float2bfloat16(f); return *(u16*)&b; }
__device__ __forceinline__ float rb(float f){ return bf2f(f2bf(f)); }
constexpr int S=2048,D=448,H=16,DH=28,F=1792;

__global__ void k_ln(const u16* x, u16* xn){
  int row=blockIdx.x; int t=threadIdx.x;
  float v[7]; float s=0;
  for(int i=0;i<7;i++){ v[i]=bf2f(x[row*D+t+64*i]); s+=v[i]; }
  for(int o=32;o;o>>=1) s+=__shfl_xor(s,o);
  float m=s/D; float q=0;
  for(int i=0;i<7;i++){ float d=v[i]-m; q+=d*d; }
  for(int o=32;o;o>>=1) q+=__shfl_xor(q,o);
  float var=rb(q/D); float mb=rb(m);
  float sd=rb(sqrtf(rb(var+1e-5f)));
  for(int i=0;i<7;i++){ xn[row*D+t+64*i]=f2bf(rb(v[i]-mb)/sd); }
}
__global__ void k_gemm(const u16* A,const u16* B,u16* C,int N,int K){
  __shared__ float As[16][17], Bs[16][17];
  int tx=threadIdx.x&15, ty=threadIdx.x>>4;
  int r=blockIdx.y*16+ty, c=blockIdx.x*16+tx; float acc=0;
  for(int k0=0;k0<K;k0+=16){
    As[ty][tx]=bf2f(A[r*K+k0+tx]); Bs[ty][tx]=bf2f(B[(k0+ty)*N+c]);
    __syncthreads();
    for(int k=0;k<16;k++) acc+=As[ty][k]*Bs[k][tx];
    __syncthreads();
  }
  C[r*N+c]=f2bf(acc);
}
__global__ __launch_bounds__(256,1) void k_phi(const u16* qkv,const u16* wphi,u16* phiq,u16* phik){
  int idx=blockIdx.x*blockDim.x+threadIdx.x;
  int w=idx&1, h=(idx>>1)&15, s=idx>>5;
  const u16* src=qkv+s*3*D+w*D+h*DH;
  float p[DH];
  #pragma unroll
  for(int f=0;f<DH;f++){ float a=0;
    #pragma unroll 1
    for(int d=0;d<DH;d++) a+=bf2f(src[d])*bf2f(wphi[(h*DH+d)*DH+f]); p[f]=rb(a);}
  float mx=-1e30f,mn=1e30f;
  #pragma unroll
  for(int f=0;f<DH;f++){mx=fmaxf(mx,p[f]);mn=fminf(mn,p[f]);}
  float sp=0,sn=0;
  #pragma unroll
  for(int f=0;f<DH;f++){sp+=expf(p[f]-mx);sn+=expf(mn-p[f]);}
  u16* dst=(w?phik:phiq)+(s*H+h)*56;
  #pragma unroll
  for(int f=0;f<DH;f++){dst[f]=f2bf(expf(p[f]-mx)/sp);dst[28+f]=f2bf(expf(mn-p[f])/sn);}
}
__global__ void k_kv(const u16* phik,const u16* qkv,float* kv){
  int h=blockIdx.x/56,f=blockIdx.x%56; int t=threadIdx.x;
  __shared__ float red[256];
  for(int d=0;d<29;d++){
    float a=0;
    for(int s=t;s<S;s+=256){ float p=bf2f(phik[(s*H+h)*56+f]); float v=d<28?bf2f(qkv[s*3*D+2*D+h*DH+d]):1.f; a+=p*v; }
    red[t]=a; __syncthreads();
    for(int o=128;o;o>>=1){ if(t<o) red[t]+=red[t+o]; __syncthreads(); }
    if(t==0) kv[(h*56+f)*29+d]=rb(red[0]);
    __syncthreads();
  }
}
__global__ void k_o(const u16* phiq,const float* kv,u16* o){
  int idx=blockIdx.x*blockDim.x+threadIdx.x;
  int s=idx/D, c=idx%D, h=c/DH, d=c%DH;
  float num=0,den=0;
  for(int f=0;f<56;f++){ float p=bf2f(phiq[(s*H+h)*56+f]); num+=p*kv[(h*56+f)*29+d]; den+=p*kv[(h*56+f)*29+28]; }
  o[idx]=f2bf(rb(num)/rb(rb(den)+1e-6f));
}
__global__ void k_h(const u16* g,const u16* u,u16* hh){
  int idx=blockIdx.x*blockDim.x+threadIdx.x;
  float a=bf2f(g[idx]); a=a>0?a:0.f*a; hh[idx]=f2bf(a*bf2f(u[idx]));
}
__global__ void k_fin(const u16* x,const u16* a,const u16* f,u16* out){
  int idx=blockIdx.x*blockDim.x+threadIdx.x;
  out[idx]=f2bf(rb(bf2f(x[idx])+bf2f(a[idx]))+bf2f(f[idx]));
}
extern "C" void kernel_launch(void* const* d_in, const int* in_sizes, int n_in, void* d_out, int out_size, void* d_ws, size_t ws_size, hipStream_t stream){
  const u16* x=(const u16*)d_in[0]; const u16* wqkv=(const u16*)d_in[1]; const u16* wphi=(const u16*)d_in[2]; const u16* wo=(const u16*)d_in[3];
  const u16* wg=(const u16*)d_in[5]; const u16* wu=(const u16*)d_in[6]; const u16* wd=(const u16*)d_in[7];
  char* w=(char*)d_ws; size_t off=0;
  auto al=[&](size_t b){ void* p=w+off; off+=(b+255)&~(size_t)255; return p;};
  u16* xn=(u16*)al(S*D*2); u16* qkv=(u16*)al(S*3*D*2); u16* phiq=(u16*)al(S*H*56*2); u16* phik=(u16*)al(S*H*56*2);
  float* kv=(float*)al(H*56*29*4); u16* o=(u16*)al(S*D*2); u16* g=(u16*)al(S*F*2); u16* u=(u16*)al(S*F*2); u16* hh=(u16*)al(S*F*2);
  u16* ao=(u16*)al(S*D*2); u16* fo=(u16*)al(S*D*2);
  k_ln<<<S,64,0,stream>>>(x,xn);
  k_gemm<<<dim3(3*D/16,S/16),256,0,stream>>>(xn,wqkv,qkv,3*D,D);
  k_phi<<<S*H*2/256,256,0,stream>>>(qkv,wphi,phiq,phik);
  k_kv<<<H*56,256,0,stream>>>(phik,qkv,kv);
  k_o<<<S*D/256,256,0,stream>>>(phiq,kv,o);
  k_gemm<<<dim3(D/16,S/16),256,0,stream>>>(o,wo,ao,D,D);
  k_gemm<<<dim3(F/16,S/16),256,0,stream>>>(xn,wg,g,F,D);
  k_gemm<<<dim3(F/16,S/16),256,0,stream>>>(xn,wu,u,F,D);
  k_h<<<S*F/256,256,0,stream>>>(g,u,hh);
  k_gemm<<<dim3(D/16,S/16),256,0,stream>>>(hh,wd,fo,D,F);
  k_fin<<<S*D/256,256,0,stream>>>(x,ao,fo,(u16*)d_out);
}
```

```cpp
#include <hip/hip_runtime.h>
#pragma clang diagnostic ignored "-Wint-to-pointer-cast"

typedef unsigned short u16;
typedef unsigned int u32;
typedef unsigned long long u64;
typedef __attribute__((ext_vector_type(8))) __bf16 bf16x8;
typedef __attribute__((ext_vector_type(2))) __bf16 bf16x2;
typedef __attribute__((ext_vector_type(4))) short s16x4;
typedef __attribute__((ext_vector_type(16))) float f32x16;
typedef __attribute__((ext_vector_type(4))) float f32x4;
typedef __attribute__((ext_vector_type(4))) u32 u32x4;
typedef __attribute__((ext_vector_type(2))) u32 u32x2;
#define LAS __attribute__((address_space(3)))
#define GAS __attribute__((address_space(1)))
#define DI __device__ __forceinline__
#define SBAR() __builtin_amdgcn_sched_barrier(0)
#define PIN(x) asm volatile("" : "+v"(x))

constexpr int S_ = 2048, D_ = 448, F_ = 1792;
constexpr size_t WS_H = 0;
constexpr size_t WS_PQ = WS_H + 64ull * 112 * 1024;
constexpr size_t WS_KVP = WS_PQ + 64ull * 16 * 2048;
constexpr size_t WS_KVF = WS_KVP + 16ull * 2 * 16 * 4096;
constexpr size_t WS_CNT = WS_KVF + 65536;
constexpr size_t WS_DBG = WS_CNT + 4096;

#define STAMP(k) do {} while (0)

DI f32x16 mfma(u32x4 a, u32x4 b, f32x16 c) {
  return __builtin_amdgcn_mfma_f32_32x32x16_bf16(__builtin_bit_cast(bf16x8, a), __builtin_bit_cast(bf16x8, b), c, 0, 0, 0);
}
DI u32 pk2(float lo, float hi) { bf16x2 p; p[0] = (__bf16)lo; p[1] = (__bf16)hi; return __builtin_bit_cast(u32, p); }
DI float rbf(float f) { return (float)(__bf16)f; }
DI float dot2(u32 a, u32 b, float c) {
  return __builtin_amdgcn_fdot2_f32_bf16(__builtin_bit_cast(bf16x2, a), __builtin_bit_cast(bf16x2, b), c, false);
}
DI u32x2 ldtr(u32 addr) {
  return __builtin_bit_cast(u32x2, __builtin_amdgcn_ds_read_tr16_b64_v4i16((LAS s16x4*)addr));
}
DI u32x4 ldtr2(u32 a0, u32 a1) { u32x2 a = ldtr(a0), b = ldtr(a1); return u32x4{a[0], a[1], b[0], b[1]}; }
DI u32x4 ld128(u32 addr) { return *(LAS u32x4*)addr; }
DI void st128(u32 addr, u32x4 v) { *(LAS u32x4*)addr = v; }
DI void st64(u32 addr, u32x2 v) { *(LAS u32x2*)addr = v; }
DI u32x4 mksrd(const void* p, u32 bytes) {
  u64 a = (u64)p;
  return u32x4{(u32)a, (u32)(a >> 32) & 0xffffu, bytes, 0x00020000u};
}
DI void dma16(u32x4 srd, u32 voff, u32 soff, u32 lds) {
  u32 keep;
  asm volatile("s_mov_b32 %0, m0\n\ts_mov_b32 m0, %4\n\ts_nop 0\n\tbuffer_load_dwordx4 %1, %2, %3 offen lds\n\ts_mov_b32 m0, %0"
               : "=&s"(keep) : "v"(voff), "s"(srd), "s"(soff), "s"(lds) : "memory");
}
#define WAITV(n) asm volatile("s_waitcnt vmcnt(%0)" ::"i"(n) : "memory")
#define BAR_L() asm volatile("s_waitcnt lgkmcnt(0)\n\ts_barrier" ::: "memory")
DI float xhalf_hi(float v) { auto r = __builtin_amdgcn_permlane32_swap(__float_as_uint(v), __float_as_uint(v), false, false); return __uint_as_float(r[1]); }
DI void xhalf(float v, float& lo, float& hi) {
  auto r = __builtin_amdgcn_permlane32_swap(__float_as_uint(v), __float_as_uint(v), false, false);
  lo = __uint_as_float(r[0]); hi = __uint_as_float(r[1]);
}
constexpr float LOG2E = 1.4426950408889634f;

constexpr u32 L1_WH = 20480, L1_X = 40960, L1_WPQ = 57344, L1_WPK = 65536 + 57344, L1_RS = L1_WPK + 2048 + 256,
              L1_PHI = 131072, L1_VF = 147456;

__global__ __launch_bounds__(512, 2) void k1(const u16* __restrict__ x, const u16* __restrict__ wqkv, const u16* __restrict__ wphi,
                                             const u16* __restrict__ wg, const u16* __restrict__ wu, char* __restrict__ ws) {
  constexpr int KID = 0;
  __shared__ __attribute__((aligned(16))) char smem[163840];
  const u32 lb = (u32)(size_t)(LAS char*)smem;
  const int wid = __builtin_amdgcn_readfirstlane(threadIdx.x >> 6);
  const int l = threadIdx.x & 63;
  const int xcd = blockIdx.x & 7, idx = blockIdx.x >> 3;
  const int panel = (xcd >> 1) * 4 + (idx & 3);
  const int j = (xcd & 1) * 8 + (idx >> 2);
  const bool ytype = (j < 2) || (j >= 14);
  const int qg = j < 2 ? j : j - 12;
  const int line0 = j < 2 ? j : (j >= 14 ? j + 12 : 2 * j - 2);
  const int koff = (j & 1) * 8;
  const int blk = (l >> 4) & 1, q = (l & 15) >> 2, p = l & 3, h = l >> 5, r = l & 31;
  const u32 rowpart = (8 * h + q) * 128, rx = 64 * ((q >> 1) & 1);
  STAMP(0);
  if (blockIdx.x == 0 && threadIdx.x < 4) *(u32*)(ws + WS_CNT + threadIdx.x * 256) = 0;

  if (wid >= 4) {
    const int w = wid - 4;
    const bool isq = ytype && w >= 2;
    const bool isu = !isq && (w & 1);
    const u32 wstride = isq ? 2688 : 3584;
    const u32x4 srdW = isq ? mksrd(wqkv, D_ * 3 * D_ * 2) : mksrd(isu ? wu : wg, D_ * F_ * 2);
    const u32x4 srdQ = mksrd(wqkv, D_ * 3 * D_ * 2);
    const u32x4 srdX = mksrd(x, S_ * D_ * 2);
    const u32 lc = (16 * (l & 7)) ^ (64 * ((l >> 4) & 1));
    const u32 qwin = qg == 3 ? 640 : 224 * qg;
    const u32 colbase = isq ? qwin + (w - 2) * 128 : (line0 + (ytype ? 0 : (w >> 1))) * 128;
    const u32 voffW = (l >> 3) * wstride + colbase + (lc ^ (isu ? 32 : 0));
    const u32 kwin = 896 + 56 * j - koff;
    const u32 voffKV = (l >> 3) * 2688 + kwin + (lc < 64 ? lc : lc + 896 - 64);
    const u32 voffX0 = (l >> 3) * 896 + 16 * ((l & 7) ^ (l >> 4));
    const u32 voffX1 = (l >> 3) * 896 + 16 * ((l & 7) ^ (4 + (l >> 4)));
    const u32 xrow0 = (panel * 128 + w * 32) * 896;
    auto issueW = [&](int s) {
      const u32 base = lb + (u32)((s >> 1) & 1) * 65536 + (u32)(s & 1) * L1_WH;
#pragma unroll
      for (int i = 0; i < 4; ++i) dma16(srdW, voffW, (32 * s + 8 * i) * wstride, base + w * 4096 + i * 1024);
      dma16(srdQ, voffKV, (32 * s + 8 * w) * 2688, base + 16384 + w * 1024);
    };
    auto issueX = [&](int i2) {
      const u32 base = lb + (u32)(i2 & 1) * 65536 + L1_X + w * 4096;
#pragma unroll
      for (int i = 0; i < 4; ++i) dma16(srdX, (i & 1) ? voffX1 : voffX0, xrow0 + i * 8 * 896 + i2 * 128, base + i * 1024);
    };
    issueX(0); issueW(0); issueW(1); issueX(1); issueW(2);
    const u32 a0 = lb + w * 4096 + rowpart + ((32 * blk + 8 * p) ^ rx);
    const u32 a1 = a0 ^ 64;
    const u32 a2 = lb + 16384 + rowpart + ((64 * (w & 1) + 32 * blk + 8 * p) ^ rx);
    const u32 ax = lb + L1_X + (32 * w + r) * 128 + 16 * (h ^ ((r >> 1) & 7));
    float c0 = 0.f, c1 = 0.f, c2 = 0.f, sx = 0.f, sq = 0.f;
    const u32 ones = 0x3f803f80u;
#pragma unroll
    for (int s = 0; s < 14; ++s) {
      if (s == 13) WAITV(0); else if (s == 12) WAITV(5); else if (s == 0 || (s & 1)) WAITV(14); else WAITV(10);
      BAR_L();
      if (s == 0) STAMP(1);
      const u32 so = (u32)((s >> 1) & 1) * 65536;
      const u32 wo_ = so + (u32)(s & 1) * L1_WH;
      u32x4 f0[2], f1[2], f2[2], fx[2];
#pragma unroll
      for (int k = 0; k < 2; ++k) {
        f0[k] = ldtr2(a0 + wo_ + k * 2048, a0 + wo_ + k * 2048 + 512);
        f1[k] = ldtr2(a1 + wo_ + k * 2048, a1 + wo_ + k * 2048 + 512);
        f2[k] = ldtr2(a2 + wo_ + k * 2048, a2 + wo_ + k * 2048 + 512);
        fx[k] = ld128((ax ^ (32 * (2 * (s & 1) + k))) + so);
      }
      if (!(s & 1) && s >= 2 && s <= 10) issueX(s / 2 + 1);
      if (s + 3 <= 13) issueW(s + 3);
      if (s == 12) {
        const u32x4 srdP = mksrd(wphi, 16 * 784 * 2);
        if (w < 2) dma16(srdP, l * 16, j * 1568 + w * 1024, lb + L1_WPK + w * 1024);
        if (ytype) {
          dma16(srdP, l * 16, qg * 6272 + w * 1024, lb + L1_WPQ + w * 1024);
          dma16(srdP, l * 16, qg * 6272 + (w + 4) * 1024, lb + L1_WPQ + (w + 4) * 1024);
        }
      }
#pragma unroll
      for (int k = 0; k < 2; ++k) {
#pragma unroll
        for (int e = 0; e < 4; ++e) {
          c0 = dot2(f0[k][e], ones, c0);
          c1 = dot2(f1[k][e], ones, c1);
          sx = dot2(fx[k][e], ones, sx);
          sq = dot2(fx[k][e], fx[k][e], sq);
          c2 = dot2(f2[k][e], ones, c2);
        }
      }
      PIN(c0); PIN(c1); PIN(c2); PIN(sx); PIN(sq);
    }
    auto putcs = [&](float c, u32 subbase, int b) {
      float lo, hi; xhalf(c, lo, hi); c = lo + hi;
      const __bf16 t0 = (__bf16)c; const float r1 = c - (float)t0;
      const __bf16 t1 = (__bf16)r1; const float r2 = r1 - (float)t1;
      const __bf16 t2 = (__bf16)r2;
      const u32 a = lb + 65536 + subbase + 64 * b + 2 * r;
      *(LAS __bf16*)(a) = t0; *(LAS __bf16*)(a + 128) = t1; *(LAS __bf16*)((a ^ 64) + 256) = t2;
    };
    putcs(c0, w * 4096, 0); putcs(c1, w * 4096, 1);
    if (w < 2) putcs(c2, 16384, w);
    {
      float lo, hi; xhalf(sx, lo, hi); const float mean = (lo + hi) * (1.f / D_);
      xhalf(sq, lo, hi); const float var = fmaxf((lo + hi) * (1.f / D_) - mean * mean, 0.f);
      const float sd = rbf(sqrtf(rbf(rbf(var) + 1e-5f)));
      const u32 mm = pk2(-mean, -mean);
      u32x4 v = {h ? 0u : mm, h ? 0u : (mm & 0xffffu), 0u, 0u};
      st128(ax + 65536, v);
      *(LAS float*)(lb + L1_RS + (32 * w + r) * 4) = 1.f / sd;
    }
    BAR_L();
    BAR_L();
    if (w < 2) {
      const u32 fq = ((q >> 1) << 3) | (h << 2) | (q & 1);
      const u32 slot = 8 * w + 4 * blk + p;
      const u32 b0 = lb + L1_PHI + (4 * h + q) * 128 + 8 * (slot ^ fq);
      const u32 b1 = lb + L1_PHI + (8 + 4 * h + q) * 128 + 8 * (slot ^ fq ^ 2);
      f32x16 acc = {};
#pragma unroll
      for (int s = 0; s < 8; ++s) acc = mfma(ldtr2(b0 + s * 2048, b1 + s * 2048), ld128(lb + L1_VF + s * 1024 + l * 16), acc);
      f32x4* dst = (f32x4*)(ws + WS_KVP + (size_t)((j * 2 + w) * 16 + panel) * 4096) + l;
#pragma unroll
      for (int g = 0; g < 4; ++g) dst[g * 64] = f32x4{acc[4 * g], acc[4 * g + 1], acc[4 * g + 2], acc[4 * g + 3]};
    }
    STAMP(4);
    return;
  }

  const int m = wid >> 1, n = wid & 1;
  const int t0 = 2 * m + n, t1 = 2 * m + 1 - n;
  u32 aw[4];
#pragma unroll
  for (int u = 0; u < 4; ++u) {
    u32 sub, cb;
    if (!ytype) { sub = 2 * n + blk; cb = (32 * u + 8 * p) ^ (32 * blk); }
    else if (u < 2) { sub = blk; cb = (32 * (2 * n + u) + 8 * p) ^ (32 * blk); }
    else { const u32 o = ((qg == 3 ? 32 : 0) + 56 * (2 * n + u - 2) + 32 * blk + 8 * p) & 255; sub = 2 + (o >> 7); cb = o & 127; }
    aw[u] = lb + sub * 4096 + rowpart + (cb ^ rx);
  }
  u32 ak = lb + 16384 + rowpart + (((koff + 32 * blk + 8 * p) & 63) ^ rx);
  u32 av = ak ^ 64;
  u32 ax0 = lb + L1_X + (32 * t0 + r) * 128 + 16 * (h ^ ((r >> 1) & 7));
  u32 ax1 = ax0 ^ 4096;
  f32x16 acc[4][2] = {}, acck = {}, accv = {};
  BAR_L();
  STAMP(1);
  u32x4 X0 = ld128(ax0), X1 = ld128(ax1), W0 = ldtr2(aw[0], aw[0] + 512), W1 = ldtr2(aw[1], aw[1] + 512), W2 = ldtr2(aw[2], aw[2] + 512),
        W3 = ldtr2(aw[3], aw[3] + 512), WK = ldtr2(ak, ak + 512), WV = ldtr2(av, av + 512);
#pragma unroll 1
  for (int it = 0; it < 7; ++it) {
#pragma unroll
    for (int st = 0; st < 4; ++st) {
      const u32 no = st == 3 ? 0u : (u32)(((st + 1) >> 1) * L1_WH + ((st + 1) & 1) * 2048);
      acc[0][0] = mfma(W0, X0, acc[0][0]); acc[0][1] = mfma(W0, X1, acc[0][1]);
      SBAR();
      if (st & 1) BAR_L();
      if (st == 3) {
#pragma unroll
        for (int u = 0; u < 4; ++u) { aw[u] ^= 65536; PIN(aw[u]); }
        ak ^= 65536; av ^= 65536; PIN(ak); PIN(av);
      }
      const u32 xx = st == 3 ? (96u | 65536u) : (u32)(32 * (st ^ (st + 1)));
      ax0 ^= xx; ax1 ^= xx; PIN(ax0); PIN(ax1);
      W0 = ldtr2(aw[0] + no, aw[0] + no + 512);
      u32x4 X0n = ld128(ax0);
      SBAR();
      acc[1][0] = mfma(W1, X0, acc[1][0]); acc[1][1] = mfma(W1, X1, acc[1][1]);
      SBAR();
      W1 = ldtr2(aw[1] + no, aw[1] + no + 512);
      u32x4 X1n = ld128(ax1);
      SBAR();
      acc[2][0] = mfma(W2, X0, acc[2][0]); acc[2][1] = mfma(W2, X1, acc[2][1]);
      SBAR();
      W2 = ldtr2(aw[2] + no, aw[2] + no + 512);
      SBAR();
      acc[3][0] = mfma(W3, X0, acc[3][0]); acc[3][1] = mfma(W3, X1, acc[3][1]);
      SBAR();
      W3 = ldtr2(aw[3] + no, aw[3] + no + 512);
      SBAR();
      acck = mfma(WK, X0, acck);
      SBAR();
      WK = ldtr2(ak + no, ak + no + 512);
      SBAR();
      accv = mfma(X0, WV, accv);
      SBAR();
      WV = ldtr2(av + no, av + no + 512);
      SBAR();
      X0 = X0n; X1 = X1n;
    }
  }
  STAMP(2);
  acck = mfma(WK, X0, acck); accv = mfma(X0, WV, accv);
  acc[0][0] = mfma(W0, X0, acc[0][0]); acc[0][1] = mfma(W0, X1, acc[0][1]);
  acc[1][0] = mfma(W1, X0, acc[1][0]); acc[1][1] = mfma(W1, X1, acc[1][1]);
  acc[2][0] = mfma(W2, X0, acc[2][0]); acc[2][1] = mfma(W2, X1, acc[2][1]);
  acc[3][0] = mfma(W3, X0, acc[3][0]); acc[3][1] = mfma(W3, X1, acc[3][1]);
  const float rs0 = *(LAS float*)(lb + L1_RS + (32 * t0 + r) * 4), rs1 = *(LAS float*)(lb + L1_RS + (32 * t1 + r) * 4);
  const u32 wpl = (4 * h + q) * 56 + 32 * blk + 8 * p;
  auto prephi = [&](const f32x16& a, float rs, u32 wp) {
    u32x4 b0, b1;
#pragma unroll
    for (int e = 0; e < 4; ++e) { b0[e] = pk2(a[2 * e] * rs, a[2 * e + 1] * rs); b1[e] = pk2(a[8 + 2 * e] * rs, a[9 + 2 * e] * rs); }
    b1[2] = h ? 0u : b1[2]; b1[3] = h ? 0u : b1[3];
    f32x16 pz = {};
    pz = mfma(ldtr2(wp, wp + 8 * 56), b0, pz);
    pz = mfma(ldtr2(wp + 16 * 56, wp + 24 * 56), b1, pz);
    return pz;
  };
  {
    f32x16 pz = prephi(acck, rs0, lb + L1_WPK + wpl);
#pragma unroll
    for (int e = 12; e < 16; ++e) pz[e] = h ? pz[0] : pz[e];
    float mx = pz[0], mn = pz[0];
#pragma unroll
    for (int e = 1; e < 16; ++e) { mx = fmaxf(mx, pz[e]); mn = fminf(mn, pz[e]); }
    float lo, hi; xhalf(mx, lo, hi); mx = fmaxf(lo, hi) * LOG2E; xhalf(mn, lo, hi); mn = fminf(lo, hi) * LOG2E;
    float ep[16], en[16], sp = 0.f, sn = 0.f;
#pragma unroll
    for (int e = 0; e < 16; ++e) {
      ep[e] = __builtin_amdgcn_exp2f(pz[e] * LOG2E - mx); en[e] = __builtin_amdgcn_exp2f(mn - pz[e] * LOG2E);
      if (e >= 12) { ep[e] = h ? 0.f : ep[e]; en[e] = h ? 0.f : en[e]; }
      sp += ep[e]; sn += en[e];
    }
    xhalf(sp, lo, hi); sp = __builtin_amdgcn_rcpf(lo + hi); xhalf(sn, lo, hi); sn = __builtin_amdgcn_rcpf(lo + hi);
    const u32 fr = (r & 5) | ((r & 2) << 2) | ((r & 8) >> 2);
    const u32 pb = lb + L1_PHI + (32 * t0 + r) * 128;
#pragma unroll
    for (int g = 0; g < 4; ++g) {
      st64(pb + 8 * ((2 * g + h) ^ fr), u32x2{pk2(ep[4 * g] * sp, ep[4 * g + 1] * sp), pk2(ep[4 * g + 2] * sp, ep[4 * g + 3] * sp)});
      st64(pb + 8 * ((8 + 2 * g + h) ^ fr), u32x2{pk2(en[4 * g] * sn, en[4 * g + 1] * sn), pk2(en[4 * g + 2] * sn, en[4 * g + 3] * sn)});
    }
    u32x4 vb[2];
#pragma unroll
    for (int g = 0; g < 4; ++g) {
      const f32x4 rv = *(LAS f32x4*)(lb + L1_RS + (32 * t0 + 8 * g + 4 * h) * 4);
      u32 a = pk2(accv[4 * g] * rv[0], accv[4 * g + 1] * rv[1]), b = pk2(accv[4 * g + 2] * rv[2], accv[4 * g + 3] * rv[3]);
      const u32 alt = r == 28 ? 0x3f803f80u : 0u;
      vb[g >> 1][2 * (g & 1)] = r < 28 ? a : alt; vb[g >> 1][2 * (g & 1) + 1] = r < 28 ? b : alt;
    }
    st128(lb + L1_VF + t0 * 2048 + l * 16, vb[0]); st128(lb + L1_VF + t0 * 2048 + 1024 + l * 16, vb[1]);
  }
  BAR_L();
  STAMP(3);
  auto ffn = [&](const f32x16& a, float rs, int tile, int unit) {
    const float r2 = rs * rs;
    u32x4 o;
#pragma unroll
    for (int e = 0; e < 4; ++e) o[e] = pk2(fmaxf(a[2 * e], 0.f) * a[8 + 2 * e] * r2, fmaxf(a[2 * e + 1], 0.f) * a[9 + 2 * e] * r2);
    *((u32x4*)(ws + WS_H + (size_t)((panel * 4 + tile) * 112 + unit) * 1024) + l) = o;
  };
  const int ub = ytype ? line0 * 4 + 2 * n : (line0 + n) * 4;
  ffn(acc[0][0], rs0, t0, ub); ffn(acc[0][1], rs1, t1, ub);
  ffn(acc[1][0], rs0, t0, ub + 1); ffn(acc[1][1], rs1, t1, ub + 1);
  if (!ytype) {
    ffn(acc[2][0], rs0, t0, ub + 2); ffn(acc[2][1], rs1, t1, ub + 2);
    ffn(acc[3][0], rs0, t0, ub + 3); ffn(acc[3][1], rs1, t1, ub + 3);
  } else {
    auto qout = [&](const f32x16& a, float rs, int tile, int c) {
      f32x16 pz = prephi(a, rs, lb + L1_WPQ + c * 1568 + wpl);
      u32x4 o0, o1;
#pragma unroll
      for (int e = 0; e < 4; ++e) { o0[e] = pk2(pz[2 * e], pz[2 * e + 1]); o1[e] = pk2(pz[8 + 2 * e], pz[9 + 2 * e]); }
      u32x4* dst = (u32x4*)(ws + WS_PQ + (size_t)((panel * 4 + tile) * 16 + qg * 4 + c) * 2048) + l;
      dst[0] = o0; dst[64] = o1;
    };
    qout(acc[2][0], rs0, t0, 2 * n); qout(acc[2][1], rs1, t1, 2 * n);
    qout(acc[3][0], rs0, t0, 2 * n + 1); qout(acc[3][1], rs1, t1, 2 * n + 1);
  }
  STAMP(4);
}

constexpr u32 L2_O = 98304;

__global__ __launch_bounds__(512, 2) void k2(const u16* __restrict__ x, const u16* __restrict__ wo, const u16* __restrict__ wd,
                                             char* __restrict__ ws, u16* __restrict__ out) {
  constexpr int KID = 1;
  __shared__ __attribute__((aligned(16))) char smem[163840];
  const u32 lb = (u32)(size_t)(LAS char*)smem;
  const int wid = __builtin_amdgcn_readfirstlane(threadIdx.x >> 6);
  const int l = threadIdx.x & 63;
  const int xcd = blockIdx.x & 7, idx = blockIdx.x >> 3;
  const int blk = (l >> 4) & 1, q = (l & 15) >> 2, p = l & 3, h = l >> 5, r = l & 31;
  STAMP(0);
  if (idx < 4) {
    if (wid >= 4) return;
    const int hf = xcd * 4 + idx;
    const f32x4* src = (const f32x4*)(ws + WS_KVP + (size_t)hf * 65536) + threadIdx.x;
    f32x4 v[16];
#pragma unroll
    for (int i = 0; i < 16; ++i) v[i] = src[i * 256];
#pragma unroll
    for (int o = 8; o; o >>= 1)
#pragma unroll
      for (int i = 0; i < o; ++i) v[i] += v[i + o];
    u64 val = (u64)pk2(v[0][0], v[0][1]) | ((u64)pk2(v[0][2], v[0][3]) << 32);
    if (r == 29) val = 0x3f803f803f803f80ull;
    __hip_atomic_store((GAS u64*)(ws + WS_KVF + hf * 2048 + (wid >> 1) * 1024 + l * 16 + (wid & 1) * 8), val, __ATOMIC_RELAXED, __HIP_MEMORY_SCOPE_AGENT);
    WAITV(0);
    BAR_L();
    if (threadIdx.x == 0) __hip_atomic_fetch_add((GAS u32*)(ws + WS_CNT + (hf >> 3) * 256), 1u, __ATOMIC_RELAXED, __HIP_MEMORY_SCOPE_AGENT);
    STAMP(4);
    return;
  }
  const int ti = xcd * 4 + ((idx - 4) & 3), ct = (idx - 4) >> 2;

  if (wid >= 4) {
    const int a = wid - 4;
    u32x4 pq[4][2][2];
#pragma unroll
    for (int c = 0; c < 4; ++c)
#pragma unroll
      for (int b = 0; b < 2; ++b) {
        const u32x4* src = (const u32x4*)(ws + WS_PQ + (size_t)((2 * ti + b) * 16 + 4 * a + c) * 2048) + l;
        pq[c][b][0] = src[0]; pq[c][b][1] = src[64];
      }
    u32x4 ef[4][2][4];
#pragma unroll
    for (int c = 0; c < 4; ++c)
#pragma unroll
      for (int b = 0; b < 2; ++b) {
        float pz[16];
#pragma unroll
        for (int e = 0; e < 8; ++e) {
          const u32 u = pq[c][b][e >> 2][e & 3];
          pz[2 * e] = __uint_as_float(u << 16); pz[2 * e + 1] = __uint_as_float(u & 0xffff0000u);
        }
#pragma unroll
        for (int e = 12; e < 16; ++e) pz[e] = h ? pz[0] : pz[e];
        float mx = pz[0], mn = pz[0];
#pragma unroll
        for (int e = 1; e < 16; ++e) { mx = fmaxf(mx, pz[e]); mn = fminf(mn, pz[e]); }
        float lo, hi; xhalf(mx, lo, hi); mx = fmaxf(lo, hi) * LOG2E; xhalf(mn, lo, hi); mn = fminf(lo, hi) * LOG2E;
#pragma unroll
        for (int e = 0; e < 8; ++e) {
          u32 up = pk2(__builtin_amdgcn_exp2f(pz[2 * e] * LOG2E - mx), __builtin_amdgcn_exp2f(pz[2 * e + 1] * LOG2E - mx));
          u32 un = pk2(__builtin_amdgcn_exp2f(mn - pz[2 * e] * LOG2E), __builtin_amdgcn_exp2f(mn - pz[2 * e + 1] * LOG2E));
          if (e >= 6) { up = h ? 0u : up; un = h ? 0u : un; }
          ef[c][b][e >> 2][e & 3] = up; ef[c][b][2 + (e >> 2)][e & 3] = un;
        }
      }
    STAMP(1);
    {
      GAS u32* cnt = (GAS u32*)(ws + WS_CNT + a * 256);
      for (int spin = 0; spin < (1 << 22); ++spin) {
        if (__builtin_amdgcn_readfirstlane(__hip_atomic_load(cnt, __ATOMIC_RELAXED, __HIP_MEMORY_SCOPE_AGENT)) >= 8u) break;
        __builtin_amdgcn_s_sleep(2);
      }
      __builtin_amdgcn_fence(__ATOMIC_ACQUIRE, "wavefront");
    }
    STAMP(2);
    const auto rsk = __builtin_amdgcn_make_buffer_rsrc((void*)(ws + WS_KVF), 0, 65536, 0x00020000);
    u32x4 kv[4][4];
#pragma unroll
    for (int c = 0; c < 4; ++c)
#pragma unroll
      for (int s = 0; s < 4; ++s) kv[c][s] = __builtin_amdgcn_raw_buffer_load_b128(rsk, l * 16, (4 * a + c) * 4096 + s * 1024, 16);
#pragma unroll
    for (int c = 0; c < 4; ++c)
#pragma unroll
      for (int b = 0; b < 2; ++b) {
        f32x16 ap = {}, an = {};
        ap = mfma(kv[c][0], ef[c][b][0], ap); an = mfma(kv[c][2], ef[c][b][2], an);
        ap = mfma(kv[c][1], ef[c][b][1], ap); an = mfma(kv[c][3], ef[c][b][3], an);
        const float rp = __builtin_amdgcn_rcpf(ap[13]), rn = __builtin_amdgcn_rcpf(an[13]);
        const float rd = __builtin_amdgcn_rcpf(ap[12] * rp + an[12] * rn + 1e-6f);
        const float ca = xhalf_hi(rp * rd), cb = xhalf_hi(rn * rd);
        u32x4 o0, o1;
#pragma unroll
        for (int e = 0; e < 4; ++e) {
          o0[e] = pk2(ap[2 * e] * ca + an[2 * e] * cb, ap[2 * e + 1] * ca + an[2 * e + 1] * cb);
          o1[e] = pk2(ap[8 + 2 * e] * ca + an[8 + 2 * e] * cb, ap[9 + 2 * e] * ca + an[9 + 2 * e] * cb);
        }
        o1[2] = h ? 0u : o1[2]; o1[3] = h ? 0u : o1[3];
        const u32 dst = lb + L2_O + (4 * a + c) * 4096 + b * 2048 + l * 16;
        st128(dst, o0); st128(dst + 1024, o1);
      }
    STAMP(3);
    BAR_L();
    return;
  }

  const int w = wid;
  const u32x4 srdH = mksrd(ws + WS_H, 64 * 112 * 1024), srdD = mksrd(wd, F_ * D_ * 2), srdO = mksrd(wo, D_ * D_ * 2);
  const u32 voffW = (l >> 3) * 896 + ct * 128 + ((16 * (l & 7)) ^ (64 * ((l >> 4) & 1)));
  const u32 voffH = l * 16;
  const u32 ring = lb + w * 24576;
  auto issue = [&](int z) {
    const u32 base = ring + (z % 3) * 8192;
    if (z < 14) {
#pragma unroll
      for (int i = 0; i < 4; ++i) dma16(srdH, voffH, ((2 * ti + (i >> 1)) * 112 + 28 * w + 2 * z + (i & 1)) * 1024, base + i * 1024);
#pragma unroll
      for (int i = 0; i < 4; ++i) dma16(srdD, voffW, (16 * (28 * w + 2 * z) + 8 * i) * 896, base + 4096 + i * 1024);
    } else {
#pragma unroll
      for (int i = 0; i < 4; ++i) dma16(srdO, voffW, (28 * (4 * w + z - 14) + 8 * i) * 896, base + 4096 + i * 1024);
    }
  };
  const size_t orow = (size_t)(ti * 64 + (w >> 1) * 32 + r) * D_ + ct * 64 + (w & 1) * 32 + 4 * h;
  u32x2 xr[4];
#pragma unroll
  for (int g = 0; g < 4; ++g) xr[g] = *(const u32x2*)(x + orow + 8 * g);
  issue(0); issue(1); issue(2);
  const u32 aw0 = ring + 4096 + (4 * h + q) * 128 + ((32 * blk + 8 * p) ^ (64 * ((q >> 1) & 1)));
  const u32 aw1 = aw0 ^ 64;
  const u32 ah = ring + l * 16;
  const u32 ao = lb + L2_O + w * 16384 + l * 16;
  f32x16 acc[2][2] = {};
#pragma unroll
  for (int z = 0; z < 18; ++z) {
    const int y1 = z + 1 > 17 ? 0 : (z + 1 < 14 ? 8 : 4), y2 = z + 2 > 17 ? 0 : (z + 2 < 14 ? 8 : 4);
    WAITV(y1 + y2);
    if (z == 0) STAMP(1);
    if (z == 14) { STAMP(2); BAR_L(); STAMP(3); }
    const u32 so = (z % 3) * 8192;
    u32x4 A[2][2], B[2][2];
#pragma unroll
    for (int s = 0; s < 2; ++s) {
      A[0][s] = ldtr2(aw0 + so + s * 2048, aw0 + so + s * 2048 + 1024);
      A[1][s] = ldtr2(aw1 + so + s * 2048, aw1 + so + s * 2048 + 1024);
#pragma unroll
      for (int b = 0; b < 2; ++b) B[b][s] = z < 14 ? ld128(ah + so + b * 2048 + s * 1024) : ld128(ao + (z - 14) * 4096 + b * 2048 + s * 1024);
    }
    if (z + 3 < 18) {
      asm volatile("s_waitcnt lgkmcnt(0)" : "+v"(A[0][0]), "+v"(A[0][1]), "+v"(A[1][0]), "+v"(A[1][1]), "+v"(B[0][0]), "+v"(B[0][1]), "+v"(B[1][0]), "+v"(B[1][1])::"memory");
      issue(z + 3);
    }
#pragma unroll
    for (int s = 0; s < 2; ++s)
#pragma unroll
      for (int a = 0; a < 2; ++a)
#pragma unroll
        for (int b = 0; b < 2; ++b) acc[a][b] = mfma(A[a][s], B[b][s], acc[a][b]);
  }
  asm volatile("s_waitcnt lgkmcnt(0)" ::: "memory");
  f32x16 mine = {};
#pragma unroll
  for (int t = 0; t < 4; ++t) {
    const f32x16& a = acc[t & 1][t >> 1];
    if (t == w) mine = a;
    else {
#pragma unroll
      for (int g = 0; g < 4; ++g)
        *(LAS f32x4*)(ring + t * 4096 + g * 1024 + l * 16) = f32x4{a[4 * g], a[4 * g + 1], a[4 * g + 2], a[4 * g + 3]};
    }
  }
  BAR_L();
#pragma unroll
  for (int o = 1; o < 4; ++o) {
    const u32 src = lb + ((w + o) & 3) * 24576 + w * 4096 + l * 16;
#pragma unroll
    for (int g = 0; g < 4; ++g) {
      const f32x4 v = *(LAS f32x4*)(src + g * 1024);
#pragma unroll
      for (int e = 0; e < 4; ++e) mine[4 * g + e] += v[e];
    }
  }
#pragma unroll
  for (int g = 0; g < 4; ++g) {
    const float x0 = __uint_as_float(xr[g][0] << 16), x1 = __uint_as_float(xr[g][0] & 0xffff0000u);
    const float x2 = __uint_as_float(xr[g][1] << 16), x3 = __uint_as_float(xr[g][1] & 0xffff0000u);
    *(u32x2*)(out + orow + 8 * g) = u32x2{pk2(mine[4 * g] + x0, mine[4 * g + 1] + x1), pk2(mine[4 * g + 2] + x2, mine[4 * g + 3] + x3)};
  }
  STAMP(4);
}

extern "C" void kernel_launch(void* const* d_in, const int* in_sizes, int n_in, void* d_out, int out_size, void* d_ws, size_t ws_size,
                              hipStream_t stream) {
  const u16* x = (const u16*)d_in[0];
  k1<<<256, 512, 0, stream>>>(x, (const u16*)d_in[1], (const u16*)d_in[2], (const u16*)d_in[5], (const u16*)d_in[6], (char*)d_ws);
  k2<<<256, 512, 0, stream>>>(x, (const u16*)d_in[3], (const u16*)d_in[7], (char*)d_ws, (u16*)d_out);
}
```

```cpp
#include <hip/hip_runtime.h>
#pragma clang diagnostic ignored "-Wint-to-pointer-cast"

typedef unsigned short u16;
typedef unsigned int u32;
typedef unsigned long long u64;
typedef __attribute__((ext_vector_type(8))) __bf16 bf16x8;
typedef __attribute__((ext_vector_type(2))) __bf16 bf16x2;
typedef __attribute__((ext_vector_type(4))) short s16x4;
typedef __attribute__((ext_vector_type(16))) float f32x16;
typedef __attribute__((ext_vector_type(4))) float f32x4;
typedef __attribute__((ext_vector_type(4))) u32 u32x4;
typedef __attribute__((ext_vector_type(2))) u32 u32x2;
#define LAS __attribute__((address_space(3)))
#define GAS __attribute__((address_space(1)))
#define DI __device__ __forceinline__
#define SBAR() __builtin_amdgcn_sched_barrier(0)
#define PIN(x) asm volatile("" : "+v"(x))

constexpr int S_ = 2048, D_ = 448, F_ = 1792;
constexpr size_t WS_H = 0;
constexpr size_t WS_PQ = WS_H + 64ull * 112 * 1024;
constexpr size_t WS_KVP = WS_PQ + 64ull * 16 * 2048;
constexpr size_t WS_KVF = WS_KVP + 16ull * 2 * 16 * 4096;
constexpr size_t WS_CNT = WS_KVF + 65536;
constexpr size_t WS_DBG = WS_CNT + 4096;

#define STAMP(k) do {} while (0)

DI f32x16 mfma(u32x4 a, u32x4 b, f32x16 c) {
  return __builtin_amdgcn_mfma_f32_32x32x16_bf16(__builtin_bit_cast(bf16x8, a), __builtin_bit_cast(bf16x8, b), c, 0, 0, 0);
}
DI u32 pk2(float lo, float hi) { bf16x2 p; p[0] = (__bf16)lo; p[1] = (__bf16)hi; return __builtin_bit_cast(u32, p); }
DI float rbf(float f) { return (float)(__bf16)f; }
DI float dot2(u32 a, u32 b, float c) {
  return __builtin_amdgcn_fdot2_f32_bf16(__builtin_bit_cast(bf16x2, a), __builtin_bit_cast(bf16x2, b), c, false);
}
DI u32x2 ldtr(u32 addr) {
  return __builtin_bit_cast(u32x2, __builtin_amdgcn_ds_read_tr16_b64_v4i16((LAS s16x4*)addr));
}
DI u32x4 ldtr2(u32 a0, u32 a1) { u32x2 a = ldtr(a0), b = ldtr(a1); return u32x4{a[0], a[1], b[0], b[1]}; }
DI u32x4 ld128(u32 addr) { return *(LAS u32x4*)addr; }
DI void st128(u32 addr, u32x4 v) { *(LAS u32x4*)addr = v; }
DI void st64(u32 addr, u32x2 v) { *(LAS u32x2*)addr = v; }
DI u32x4 mksrd(const void* p, u32 bytes) {
  u64 a = (u64)p;
  return u32x4{(u32)a, (u32)(a >> 32) & 0xffffu, bytes, 0x00020000u};
}
DI void dma16(u32x4 srd, u32 voff, u32 soff, u32 lds) {
  u32 keep;
  asm volatile("s_mov_b32 %0, m0\n\ts_mov_b32 m0, %4\n\ts_nop 0\n\tbuffer_load_dwordx4 %1, %2, %3 offen lds\n\ts_mov_b32 m0, %0"
               : "=&s"(keep) : "v"(voff), "s"(srd), "s"(soff), "s"(lds) : "memory");
}
#define WAITV(n) asm volatile("s_waitcnt vmcnt(%0)" ::"i"(n) : "memory")
#define BAR_L() asm volatile("s_waitcnt lgkmcnt(0)\n\ts_barrier" ::: "memory")
DI float xhalf_hi(float v) { auto r = __builtin_amdgcn_permlane32_swap(__float_as_uint(v), __float_as_uint(v), false, false); return __uint_as_float(r[1]); }
DI void xhalf(float v, float& lo, float& hi) {
  auto r = __builtin_amdgcn_permlane32_swap(__float_as_uint(v), __float_as_uint(v), false, false);
  lo = __uint_as_float(r[0]); hi = __uint_as_float(r[1]);
}
constexpr float LOG2E = 1.4426950408889634f;

constexpr u32 L1_WH = 20480, L1_X = 40960, L1_WPQ = 57344, L1_WPK = 65536 + 57344, L1_RS = L1_WPK + 2048 + 256,
              L1_PHI = 131072, L1_VF = 147456;

__global__ __launch_bounds__(512, 2) void k1(const u16* __restrict__ x, const u16* __restrict__ wqkv, const u16* __restrict__ wphi,
                                             const u16* __restrict__ wg, const u16* __restrict__ wu, char* __restrict__ ws) {
  constexpr int KID = 0;
  __shared__ __attribute__((aligned(16))) char smem[163840];
  const u32 lb = (u32)(size_t)(LAS char*)smem;
  const int wid = __builtin_amdgcn_readfirstlane(threadIdx.x >> 6);
  const int l = threadIdx.x & 63;
  const int xcd = blockIdx.x & 7, idx = blockIdx.x >> 3;
  const int panel = (xcd >> 1) * 4 + (idx & 3);
  const int j = (xcd & 1) * 8 + (idx >> 2);
  const bool ytype = (j < 2) || (j >= 14);
  const int qg = j < 2 ? j : j - 12;
  const int line0 = j < 2 ? j : (j >= 14 ? j + 12 : 2 * j - 2);
  const int koff = (j & 1) * 8;
  const int blk = (l >> 4) & 1, q = (l & 15) >> 2, p = l & 3, h = l >> 5, r = l & 31;
  const u32 rowpart = (8 * h + q) * 128, rx = 64 * ((q >> 1) & 1);
  STAMP(0);
  if (blockIdx.x == 0 && threadIdx.x < 4) *(u32*)(ws + WS_CNT + threadIdx.x * 256) = 0;

  if (wid >= 4) {
    const int w = wid - 4;
    const bool isq = ytype && w >= 2;
    const bool isu = !isq && (w & 1);
    const u32 wstride = isq ? 2688 : 3584;
    const u32x4 srdW = isq ? mksrd(wqkv, D_ * 3 * D_ * 2) : mksrd(isu ? wu : wg, D_ * F_ * 2);
    const u32x4 srdQ = mksrd(wqkv, D_ * 3 * D_ * 2);
    const u32x4 srdX = mksrd(x, S_ * D_ * 2);
    const u32 lc = (16 * (l & 7)) ^ (64 * ((l >> 4) & 1));
    const u32 qwin = qg == 3 ? 640 : 224 * qg;
    const u32 colbase = isq ? qwin + (w - 2) * 128 : (line0 + (ytype ? 0 : (w >> 1))) * 128;
    const u32 voffW = (l >> 3) * wstride + colbase + (lc ^ (isu ? 32 : 0));
    const u32 kwin = 896 + 56 * j - koff;
    const u32 voffKV = (l >> 3) * 2688 + kwin + (lc < 64 ? lc : lc + 896 - 64);
    const u32 voffX0 = (l >> 3) * 896 + 16 * ((l & 7) ^ (l >> 4));
    const u32 voffX1 = (l >> 3) * 896 + 16 * ((l & 7) ^ (4 + (l >> 4)));
    const u32 xrow0 = (panel * 128 + w * 32) * 896;
    auto issueW = [&](int s) {
      const u32 base = lb + (u32)((s >> 1) & 1) * 65536 + (u32)(s & 1) * L1_WH;
#pragma unroll
      for (int i = 0; i < 4; ++i) dma16(srdW, voffW, (32 * s + 8 * i) * wstride, base + w * 4096 + i * 1024);
      dma16(srdQ, voffKV, (32 * s + 8 * w) * 2688, base + 16384 + w * 1024);
    };
    auto issueX = [&](int i2) {
      const u32 base = lb + (u32)(i2 & 1) * 65536 + L1_X + w * 4096;
#pragma unroll
      for (int i = 0; i < 4; ++i) dma16(srdX, (i & 1) ? voffX1 : voffX0, xrow0 + i * 8 * 896 + i2 * 128, base + i * 1024);
    };
    issueX(0); issueW(0); issueW(1); issueX(1); issueW(2);
    const u32 a0 = lb + w * 4096 + rowpart + ((32 * blk + 8 * p) ^ rx);
    const u32 a1 = a0 ^ 64;
    const u32 a2 = lb + 16384 + rowpart + ((64 * (w & 1) + 32 * blk + 8 * p) ^ rx);
    const u32 ax = lb + L1_X + (32 * w + r) * 128 + 16 * (h ^ ((r >> 1) & 7));
    float c0 = 0.f, c1 = 0.f, c2 = 0.f, sx = 0.f, sq = 0.f;
    const u32 ones = 0x3f803f80u;
#pragma unroll
    for (int s = 0; s < 14; ++s) {
      if (s == 13) WAITV(0); else if (s == 12) WAITV(5); else if (s == 0 || (s & 1)) WAITV(14); else WAITV(10);
      BAR_L();
      if (s == 0) STAMP(1);
      const u32 so = (u32)((s >> 1) & 1) * 65536;
      const u32 wo_ = so + (u32)(s & 1) * L1_WH;
      u32x4 f0[2], f1[2], f2[2], fx[2];
#pragma unroll
      for (int k = 0; k < 2; ++k) {
        f0[k] = ldtr2(a0 + wo_ + k * 2048, a0 + wo_ + k * 2048 + 512);
        f1[k] = ldtr2(a1 + wo_ + k * 2048, a1 + wo_ + k * 2048 + 512);
        f2[k] = ldtr2(a2 + wo_ + k * 2048, a2 + wo_ + k * 2048 + 512);
        fx[k] = ld128((ax ^ (32 * (2 * (s & 1) + k))) + so);
      }
      if (!(s & 1) && s >= 2 && s <= 10) issueX(s / 2 + 1);
      if (s + 3 <= 13) issueW(s + 3);
      if (s == 12) {
        const u32x4 srdP = mksrd(wphi, 16 * 784 * 2);
        if (w < 2) dma16(srdP, l * 16, j * 1568 + w * 1024, lb + L1_WPK + w * 1024);
        if (ytype) {
          dma16(srdP, l * 16, qg * 6272 + w * 1024, lb + L1_WPQ + w * 1024);
          dma16(srdP, l * 16, qg * 6272 + (w + 4) * 1024, lb + L1_WPQ + (w + 4) * 1024);
        }
      }
#pragma unroll
      for (int k = 0; k < 2; ++k) {
#pragma unroll
        for (int e = 0; e < 4; ++e) {
          c0 = dot2(f0[k][e], ones, c0);
          c1 = dot2(f1[k][e], ones, c1);
          sx = dot2(fx[k][e], ones, sx);
          sq = dot2(fx[k][e], fx[k][e], sq);
          c2 = dot2(f2[k][e], ones, c2);
        }
      }
      PIN(c0); PIN(c1); PIN(c2); PIN(sx); PIN(sq);
    }
    auto putcs = [&](float c, u32 subbase, int b) {
      float lo, hi; xhalf(c, lo, hi); c = lo + hi;
      const __bf16 t0 = (__bf16)c; const float r1 = c - (float)t0;
      const __bf16 t1 = (__bf16)r1; const float r2 = r1 - (float)t1;
      const __bf16 t2 = (__bf16)r2;
      const u32 a = lb + 65536 + subbase + 64 * b + 2 * r;
      *(LAS __bf16*)(a) = t0; *(LAS __bf16*)(a + 128) = t1; *(LAS __bf16*)((a ^ 64) + 256) = t2;
    };
    putcs(c0, w * 4096, 0); putcs(c1, w * 4096, 1);
    if (w < 2) putcs(c2, 16384, w);
    {
      float lo, hi; xhalf(sx, lo, hi); const float mean = (lo + hi) * (1.f / D_);
      xhalf(sq, lo, hi); const float var = fmaxf((lo + hi) * (1.f / D_) - mean * mean, 0.f);
      const float sd = rbf(sqrtf(rbf(rbf(var) + 1e-5f)));
      const u32 mm = pk2(-mean, -mean);
      u32x4 v = {h ? 0u : mm, h ? 0u : (mm & 0xffffu), 0u, 0u};
      st128(ax + 65536, v);
      *(LAS float*)(lb + L1_RS + (32 * w + r) * 4) = 1.f / sd;
    }
    BAR_L();
    BAR_L();
    if (w < 2) {
      const u32 fq = ((q >> 1) << 3) | (h << 2) | (q & 1);
      const u32 slot = 8 * w + 4 * blk + p;
      const u32 b0 = lb + L1_PHI + (4 * h + q) * 128 + 8 * (slot ^ fq);
      const u32 b1 = lb + L1_PHI + (8 + 4 * h + q) * 128 + 8 * (slot ^ fq ^ 2);
      f32x16 acc = {};
#pragma unroll
      for (int s = 0; s < 8; ++s) acc = mfma(ldtr2(b0 + s * 2048, b1 + s * 2048), ld128(lb + L1_VF + s * 1024 + l * 16), acc);
      f32x4* dst = (f32x4*)(ws + WS_KVP + (size_t)((j * 2 + w) * 16 + panel) * 4096) + l;
#pragma unroll
      for (int g = 0; g < 4; ++g) dst[g * 64] = f32x4{acc[4 * g], acc[4 * g + 1], acc[4 * g + 2], acc[4 * g + 3]};
    }
    STAMP(4);
    return;
  }

  const int m = wid >> 1, n = wid & 1;
  const int t0 = 2 * m + n, t1 = 2 * m + 1 - n;
  u32 aw[4];
#pragma unroll
  for (int u = 0; u < 4; ++u) {
    u32 sub, cb;
    if (!ytype) { sub = 2 * n + blk; cb = (32 * u + 8 * p) ^ (32 * blk); }
    else if (u < 2) { sub = blk; cb = (32 * (2 * n + u) + 8 * p) ^ (32 * blk); }
    else { const u32 o = ((qg == 3 ? 32 : 0) + 56 * (2 * n + u - 2) + 32 * blk + 8 * p) & 255; sub = 2 + (o >> 7); cb = o & 127; }
    aw[u] = lb + sub * 4096 + rowpart + (cb ^ rx);
  }
  u32 ak = lb + 16384 + rowpart + (((koff + 32 * blk + 8 * p) & 63) ^ rx);
  u32 av = ak ^ 64;
  u32 ax0 = lb + L1_X + (32 * t0 + r) * 128 + 16 * (h ^ ((r >> 1) & 7));
  u32 ax1 = ax0 ^ 4096;
  f32x16 acc[4][2] = {}, acck = {}, accv = {};
  BAR_L();
  STAMP(1);
  u32x4 X0 = ld128(ax0), X1 = ld128(ax1), W0 = ldtr2(aw[0], aw[0] + 512), W1 = ldtr2(aw[1], aw[1] + 512), W2 = ldtr2(aw[2], aw[2] + 512),
        W3 = ldtr2(aw[3], aw[3] + 512), WK = ldtr2(ak, ak + 512), WV = ldtr2(av, av + 512);
#pragma unroll 1
  for (int it = 0; it < 7; ++it) {
#pragma unroll
    for (int st = 0; st < 4; ++st) {
      const u32 no = st == 3 ? 0u : (u32)(((st + 1) >> 1) * L1_WH + ((st + 1) & 1) * 2048);
      acc[0][0] = mfma(W0, X0, acc[0][0]); acc[0][1] = mfma(W0, X1, acc[0][1]);
      SBAR();
      if (st & 1) BAR_L();
      if (st == 3) {
#pragma unroll
        for (int u = 0; u < 4; ++u) { aw[u] ^= 65536; PIN(aw[u]); }
        ak ^= 65536; av ^= 65536; PIN(ak); PIN(av);
      }
      const u32 xx = st == 3 ? (96u | 65536u) : (u32)(32 * (st ^ (st + 1)));
      ax0 ^= xx; ax1 ^= xx; PIN(ax0); PIN(ax1);
      W0 = ldtr2(aw[0] + no, aw[0] + no + 512);
      u32x4 X0n = ld128(ax0);
      SBAR();
      acc[1][0] = mfma(W1, X0, acc[1][0]); acc[1][1] = mfma(W1, X1, acc[1][1]);
      SBAR();
      W1 = ldtr2(aw[1] + no, aw[1] + no + 512);
      u32x4 X1n = ld128(ax1);
      SBAR();
      acc[2][0] = mfma(W2, X0, acc[2][0]); acc[2][1] = mfma(W2, X1, acc[2][1]);
      SBAR();
      W2 = ldtr2(aw[2] + no, aw[2] + no + 512);
      SBAR();
      acc[3][0] = mfma(W3, X0, acc[3][0]); acc[3][1] = mfma(W3, X1, acc[3][1]);
      SBAR();
      W3 = ldtr2(aw[3] + no, aw[3] + no + 512);
      SBAR();
      acck = mfma(WK, X0, acck);
      SBAR();
      WK = ldtr2(ak + no, ak + no + 512);
      SBAR();
      accv = mfma(X0, WV, accv);
      SBAR();
      WV = ldtr2(av + no, av + no + 512);
      SBAR();
      X0 = X0n; X1 = X1n;
    }
  }
  STAMP(2);
  acck = mfma(WK, X0, acck); accv = mfma(X0, WV, accv);
  acc[0][0] = mfma(W0, X0, acc[0][0]); acc[0][1] = mfma(W0, X1, acc[0][1]);
  acc[1][0] = mfma(W1, X0, acc[1][0]); acc[1][1] = mfma(W1, X1, acc[1][1]);
  acc[2][0] = mfma(W2, X0, acc[2][0]); acc[2][1] = mfma(W2, X1, acc[2][1]);
  acc[3][0] = mfma(W3, X0, acc[3][0]); acc[3][1] = mfma(W3, X1, acc[3][1]);
  const float rs0 = *(LAS float*)(lb + L1_RS + (32 * t0 + r) * 4), rs1 = *(LAS float*)(lb + L1_RS + (32 * t1 + r) * 4);
  const u32 wpl = (4 * h + q) * 56 + 32 * blk + 8 * p;
  auto prephi = [&](const f32x16& a, float rs, u32 wp) {
    u32x4 b0, b1;
#pragma unroll
    for (int e = 0; e < 4; ++e) { b0[e] = pk2(a[2 * e] * rs, a[2 * e + 1] * rs); b1[e] = pk2(a[8 + 2 * e] * rs, a[9 + 2 * e] * rs); }
    b1[2] = h ? 0u : b1[2]; b1[3] = h ? 0u : b1[3];
    f32x16 pz = {};
    pz = mfma(ldtr2(wp, wp + 8 * 56), b0, pz);
    pz = mfma(ldtr2(wp + 16 * 56, wp + 24 * 56), b1, pz);
    return pz;
  };
  {
    f32x16 pz = prephi(acck, rs0, lb + L1_WPK + wpl);
#pragma unroll
    for (int e = 12; e < 16; ++e) pz[e] = h ? pz[0] : pz[e];
    float mx = pz[0], mn = pz[0];
#pragma unroll
    for (int e = 1; e < 16; ++e) { mx = fmaxf(mx, pz[e]); mn = fminf(mn, pz[e]); }
    float lo, hi; xhalf(mx, lo, hi); mx = fmaxf(lo, hi) * LOG2E; xhalf(mn, lo, hi); mn = fminf(lo, hi) * LOG2E;
    float ep[16], en[16], sp = 0.f, sn = 0.f;
#pragma unroll
    for (int e = 0; e < 16; ++e) {
      ep[e] = __builtin_amdgcn_exp2f(pz[e] * LOG2E - mx); en[e] = __builtin_amdgcn_exp2f(mn - pz[e] * LOG2E);
      if (e >= 12) { ep[e] = h ? 0.f : ep[e]; en[e] = h ? 0.f : en[e]; }
      sp += ep[e]; sn += en[e];
    }
    xhalf(sp, lo, hi); sp = __builtin_amdgcn_rcpf(lo + hi); xhalf(sn, lo, hi); sn = __builtin_amdgcn_rcpf(lo + hi);
    const u32 fr = (r & 5) | ((r & 2) << 2) | ((r & 8) >> 2);
    const u32 pb = lb + L1_PHI + (32 * t0 + r) * 128;
#pragma unroll
    for (int g = 0; g < 4; ++g) {
      st64(pb + 8 * ((2 * g + h) ^ fr), u32x2{pk2(ep[4 * g] * sp, ep[4 * g + 1] * sp), pk2(ep[4 * g + 2] * sp, ep[4 * g + 3] * sp)});
      st64(pb + 8 * ((8 + 2 * g + h) ^ fr), u32x2{pk2(en[4 * g] * sn, en[4 * g + 1] * sn), pk2(en[4 * g + 2] * sn, en[4 * g + 3] * sn)});
    }
    u32x4 vb[2];
#pragma unroll
    for (int g = 0; g < 4; ++g) {
      const f32x4 rv = *(LAS f32x4*)(lb + L1_RS + (32 * t0 + 8 * g + 4 * h) * 4);
      u32 a = pk2(accv[4 * g] * rv[0], accv[4 * g + 1] * rv[1]), b = pk2(accv[4 * g + 2] * rv[2], accv[4 * g + 3] * rv[3]);
      const u32 alt = r == 28 ? 0x3f803f80u : 0u;
      vb[g >> 1][2 * (g & 1)] = r < 28 ? a : alt; vb[g >> 1][2 * (g & 1) + 1] = r < 28 ? b : alt;
    }
    st128(lb + L1_VF + t0 * 2048 + l * 16, vb[0]); st128(lb + L1_VF + t0 * 2048 + 1024 + l * 16, vb[1]);
  }
  BAR_L();
  STAMP(3);
  auto ffn = [&](const f32x16& a, float rs, int tile, int unit) {
    const float r2 = rs * rs;
    u32x4 o;
#pragma unroll
    for (int e = 0; e < 4; ++e) o[e] = pk2(fmaxf(a[2 * e], 0.f) * a[8 + 2 * e] * r2, fmaxf(a[2 * e + 1], 0.f) * a[9 + 2 * e] * r2);
    *((u32x4*)(ws + WS_H + (size_t)((panel * 4 + tile) * 112 + unit) * 1024) + l) = o;
  };
  const int ub = ytype ? line0 * 4 + 2 * n : (line0 + n) * 4;
  ffn(acc[0][0], rs0, t0, ub); ffn(acc[0][1], rs1, t1, ub);
  ffn(acc[1][0], rs0, t0, ub + 1); ffn(acc[1][1], rs1, t1, ub + 1);
  if (!ytype) {
    ffn(acc[2][0], rs0, t0, ub + 2); ffn(acc[2][1], rs1, t1, ub + 2);
    ffn(acc[3][0], rs0, t0, ub + 3); ffn(acc[3][1], rs1, t1, ub + 3);
  } else {
    auto qout = [&](const f32x16& a, float rs, int tile, int c) {
      f32x16 pz = prephi(a, rs, lb + L1_WPQ + c * 1568 + wpl);
      u32x4 o0, o1;
#pragma unroll
      for (int e = 0; e < 4; ++e) { o0[e] = pk2(pz[2 * e], pz[2 * e + 1]); o1[e] = pk2(pz[8 + 2 * e], pz[9 + 2 * e]); }
      u32x4* dst = (u32x4*)(ws + WS_PQ + (size_t)((panel * 4 + tile) * 16 + qg * 4 + c) * 2048) + l;
      dst[0] = o0; dst[64] = o1;
    };
    qout(acc[2][0], rs0, t0, 2 * n); qout(acc[2][1], rs1, t1, 2 * n);
    qout(acc[3][0], rs0, t0, 2 * n + 1); qout(acc[3][1], rs1, t1, 2 * n + 1);
  }
  STAMP(4);
}

constexpr u32 L2_O = 98304;

__global__ __launch_bounds__(512, 2) void k2(const u16* __restrict__ x, const u16* __restrict__ wo, const u16* __restrict__ wd,
                                             char* __restrict__ ws, u16* __restrict__ out) {
  constexpr int KID = 1;
  __shared__ __attribute__((aligned(16))) char smem[163840];
  const u32 lb = (u32)(size_t)(LAS char*)smem;
  const int wid = __builtin_amdgcn_readfirstlane(threadIdx.x >> 6);
  const int l = threadIdx.x & 63;
  const int xcd = blockIdx.x & 7, idx = blockIdx.x >> 3;
  const int blk = (l >> 4) & 1, q = (l & 15) >> 2, p = l & 3, h = l >> 5, r = l & 31;
  STAMP(0);
  if (idx < 4) {
    if (wid >= 4) return;
    const int hf = xcd * 4 + idx;
    const f32x4* src = (const f32x4*)(ws + WS_KVP + (size_t)hf * 65536) + threadIdx.x;
    f32x4 v[16];
#pragma unroll
    for (int i = 0; i < 16; ++i) v[i] = src[i * 256];
#pragma unroll
    for (int o = 8; o; o >>= 1)
#pragma unroll
      for (int i = 0; i < o; ++i) v[i] += v[i + o];
    u64 val = (u64)pk2(v[0][0], v[0][1]) | ((u64)pk2(v[0][2], v[0][3]) << 32);
    if (r == 29) val = 0x3f803f803f803f80ull;
    __hip_atomic_store((GAS u64*)(ws + WS_KVF + hf * 2048 + (wid >> 1) * 1024 + l * 16 + (wid & 1) * 8), val, __ATOMIC_RELAXED, __HIP_MEMORY_SCOPE_AGENT);
    WAITV(0);
    BAR_L();
    if (threadIdx.x == 0) __hip_atomic_fetch_add((GAS u32*)(ws + WS_CNT + (hf >> 3) * 256), 1u, __ATOMIC_RELAXED, __HIP_MEMORY_SCOPE_AGENT);
    STAMP(4);
    return;
  }
  const int ti = xcd * 4 + ((idx - 4) & 3), ct = (idx - 4) >> 2;

  if (wid >= 4) {
    const int a = wid - 4;
    u32x4 pq[4][2][2];
#pragma unroll
    for (int c = 0; c < 4; ++c)
#pragma unroll
      for (int b = 0; b < 2; ++b) {
        const u32x4* src = (const u32x4*)(ws + WS_PQ + (size_t)((2 * ti + b) * 16 + 4 * a + c) * 2048) + l;
        pq[c][b][0] = src[0]; pq[c][b][1] = src[64];
      }
    u32x4 ef[4][2][4];
#pragma unroll
    for (int c = 0; c < 4; ++c)
#pragma unroll
      for (int b = 0; b < 2; ++b) {
        float pz[16];
#pragma unroll
        for (int e = 0; e < 8; ++e) {
          const u32 u = pq[c][b][e >> 2][e & 3];
          pz[2 * e] = __uint_as_float(u << 16); pz[2 * e + 1] = __uint_as_float(u & 0xffff0000u);
        }
#pragma unroll
        for (int e = 12; e < 16; ++e) pz[e] = h ? pz[0] : pz[e];
        float mx = pz[0], mn = pz[0];
#pragma unroll
        for (int e = 1; e < 16; ++e) { mx = fmaxf(mx, pz[e]); mn = fminf(mn, pz[e]); }
        float lo, hi; xhalf(mx, lo, hi); mx = fmaxf(lo, hi) * LOG2E; xhalf(mn, lo, hi); mn = fminf(lo, hi) * LOG2E;
#pragma unroll
        for (int e = 0; e < 8; ++e) {
          u32 up = pk2(__builtin_amdgcn_exp2f(pz[2 * e] * LOG2E - mx), __builtin_amdgcn_exp2f(pz[2 * e + 1] * LOG2E - mx));
          u32 un = pk2(__builtin_amdgcn_exp2f(mn - pz[2 * e] * LOG2E), __builtin_amdgcn_exp2f(mn - pz[2 * e + 1] * LOG2E));
          if (e >= 6) { up = h ? 0u : up; un = h ? 0u : un; }
          ef[c][b][e >> 2][e & 3] = up; ef[c][b][2 + (e >> 2)][e & 3] = un;
        }
      }
    STAMP(1);
    {
      GAS u32* cnt = (GAS u32*)(ws + WS_CNT + a * 256);
      for (int spin = 0; spin < (1 << 22); ++spin) {
        if (__builtin_amdgcn_readfirstlane(__hip_atomic_load(cnt, __ATOMIC_RELAXED, __HIP_MEMORY_SCOPE_AGENT)) >= 8u) break;
        __builtin_amdgcn_s_sleep(2);
      }
      __builtin_amdgcn_fence(__ATOMIC_ACQUIRE, "wavefront");
    }
    STAMP(2);
    const auto rsk = __builtin_amdgcn_make_buffer_rsrc((void*)(ws + WS_KVF), 0, 65536, 0x00020000);
    u32x4 kv[4][4];
#pragma unroll
    for (int c = 0; c < 4; ++c)
#pragma unroll
      for (int s = 0; s < 4; ++s) kv[c][s] = __builtin_amdgcn_raw_buffer_load_b128(rsk, l * 16, (4 * a + c) * 4096 + s * 1024, 16);
#pragma unroll
    for (int c = 0; c < 4; ++c)
#pragma unroll
      for (int b = 0; b < 2; ++b) {
        f32x16 ap = {}, an = {};
        ap = mfma(kv[c][0], ef[c][b][0], ap); an = mfma(kv[c][2], ef[c][b][2], an);
        ap = mfma(kv[c][1], ef[c][b][1], ap); an = mfma(kv[c][3], ef[c][b][3], an);
        const float rp = __builtin_amdgcn_rcpf(ap[13]), rn = __builtin_amdgcn_rcpf(an[13]);
        const float rd = __builtin_amdgcn_rcpf(ap[12] * rp + an[12] * rn + 1e-6f);
        const float ca = xhalf_hi(rp * rd), cb = xhalf_hi(rn * rd);
        u32x4 o0, o1;
#pragma unroll
        for (int e = 0; e < 4; ++e) {
          o0[e] = pk2(ap[2 * e] * ca + an[2 * e] * cb, ap[2 * e + 1] * ca + an[2 * e + 1] * cb);
          o1[e] = pk2(ap[8 + 2 * e] * ca + an[8 + 2 * e] * cb, ap[9 + 2 * e] * ca + an[9 + 2 * e] * cb);
        }
        o1[2] = h ? 0u : o1[2]; o1[3] = h ? 0u : o1[3];
        const u32 dst = lb + L2_O + (4 * a + c) * 4096 + b * 2048 + l * 16;
        st128(dst, o0); st128(dst + 1024, o1);
      }
    STAMP(3);
    BAR_L();
    return;
  }

  const int w = wid;
  const u32x4 srdH = mksrd(ws + WS_H, 64 * 112 * 1024), srdD = mksrd(wd, F_ * D_ * 2), srdO = mksrd(wo, D_ * D_ * 2);
  const u32 voffW = (l >> 3) * 896 + ct * 128 + ((16 * (l & 7)) ^ (64 * ((l >> 4) & 1)));
  const u32 voffH = l * 16;
  const u32 ring = lb + w * 24576;
  auto issue = [&](int z) {
    const u32 base = ring + (z % 3) * 8192;
    if (z < 14) {
#pragma unroll
      for (int i = 0; i < 4; ++i) dma16(srdH, voffH, ((2 * ti + (i >> 1)) * 112 + 28 * w + 2 * z + (i & 1)) * 1024, base + i * 1024);
#pragma unroll
      for (int i = 0; i < 4; ++i) dma16(srdD, voffW, (16 * (28 * w + 2 * z) + 8 * i) * 896, base + 4096 + i * 1024);
    } else {
#pragma unroll
      for (int i = 0; i < 4; ++i) dma16(srdO, voffW, (28 * (4 * w + z - 14) + 8 * i) * 896, base + 4096 + i * 1024);
    }
  };
  const size_t orow = (size_t)(ti * 64 + (w >> 1) * 32 + r) * D_ + ct * 64 + (w & 1) * 32 + 4 * h;
  u32x2 xr[4];
#pragma unroll
  for (int g = 0; g < 4; ++g) xr[g] = *(const u32x2*)(x + orow + 8 * g);
  issue(0); issue(1); issue(2);
  const u32 aw0 = ring + 4096 + (4 * h + q) * 128 + ((32 * blk + 8 * p) ^ (64 * ((q >> 1) & 1)));
  const u32 aw1 = aw0 ^ 64;
  const u32 ah = ring + l * 16;
  const u32 ao = lb + L2_O + w * 16384 + l * 16;
  f32x16 acc[2][2] = {};
#pragma unroll
  for (int z = 0; z < 18; ++z) {
    const int y1 = z + 1 > 17 ? 0 : (z + 1 < 14 ? 8 : 4), y2 = z + 2 > 17 ? 0 : (z + 2 < 14 ? 8 : 4);
    WAITV(y1 + y2);
    if (z == 0) STAMP(1);
    if (z == 14) { STAMP(2); BAR_L(); STAMP(3); }
    const u32 so = (z % 3) * 8192;
    u32x4 A[2][2], B[2][2];
#pragma unroll
    for (int s = 0; s < 2; ++s) {
      A[0][s] = ldtr2(aw0 + so + s * 2048, aw0 + so + s * 2048 + 1024);
      A[1][s] = ldtr2(aw1 + so + s * 2048, aw1 + so + s * 2048 + 1024);
#pragma unroll
      for (int b = 0; b < 2; ++b) B[b][s] = z < 14 ? ld128(ah + so + b * 2048 + s * 1024) : ld128(ao + (z - 14) * 4096 + b * 2048 + s * 1024);
    }
    if (z + 3 < 18) {
      asm volatile("s_waitcnt lgkmcnt(0)" : "+v"(A[0][0]), "+v"(A[0][1]), "+v"(A[1][0]), "+v"(A[1][1]), "+v"(B[0][0]), "+v"(B[0][1]), "+v"(B[1][0]), "+v"(B[1][1])::"memory");
      issue(z + 3);
    }
#pragma unroll
    for (int s = 0; s < 2; ++s)
#pragma unroll
      for (int a = 0; a < 2; ++a)
#pragma unroll
        for (int b = 0; b < 2; ++b) acc[a][b] = mfma(A[a][s], B[b][s], acc[a][b]);
  }
  asm volatile("s_waitcnt lgkmcnt(0)" ::: "memory");
  f32x16 mine = {};
#pragma unroll
  for (int t = 0; t < 4; ++t) {
    const f32x16& a = acc[t & 1][t >> 1];
    if (t == w) mine = a;
    else {
#pragma unroll
      for (int g = 0; g < 4; ++g)
        *(LAS f32x4*)(ring + t * 4096 + g * 1024 + l * 16) = f32x4{a[4 * g], a[4 * g + 1], a[4 * g + 2], a[4 * g + 3]};
    }
  }
  BAR_L();
#pragma unroll
  for (int o = 1; o < 4; ++o) {
    const u32 src = lb + ((w + o) & 3) * 24576 + w * 4096 + l * 16;
#pragma unroll
    for (int g = 0; g < 4; ++g) {
      const f32x4 v = *(LAS f32x4*)(src + g * 1024);
#pragma unroll
      for (int e = 0; e < 4; ++e) mine[4 * g + e] += v[e];
    }
  }
#pragma unroll
  for (int g = 0; g < 4; ++g) {
    const float x0 = __uint_as_float(xr[g][0] << 16), x1 = __uint_as_float(xr[g][0] & 0xffff0000u);
    const float x2 = __uint_as_float(xr[g][1] << 16), x3 = __uint_as_float(xr[g][1] & 0xffff0000u);
    *(u32x2*)(out + orow + 8 * g) = u32x2{pk2(mine[4 * g] + x0, mine[4 * g + 1] + x1), pk2(mine[4 * g + 2] + x2, mine[4 * g + 3] + x3)};
  }
  STAMP(4);
}

struct PP { const char* p[10]; };
template<int STRIDE, int MASK>
__global__ void kpre(const char* p0,const char* p1,const char* p2,const char* p3,const char* p5,const char* p6,const char* p7,const char* ws,const char* out, char* sink){
  u64 t0=__builtin_amdgcn_s_memrealtime();
  const int idx=blockIdx.x>>3; u32 acc=0;
  auto touch=[&](const char* p, u32 n){ for(u32 o=(idx*256+threadIdx.x)*STRIDE;o<n;o+=32*256*STRIDE) acc+=*(const volatile u32*)(p+o); };
  if(MASK&1){ touch(p0,2048*448*2); touch(p1,448*1344*2); touch(p2,16*784*2); touch(p5,448*1792*2); touch(p6,448*1792*2);}
  if(MASK&2){ touch(p3,448*448*2); touch(p7,448*1792*2);}
  if(MASK&4){ touch(ws,(u32)WS_DBG); touch(out,2048*448*2);}
  if(acc==0x12345678) sink[WS_DBG+64]=1;
  while(__builtin_amdgcn_s_memrealtime()-t0<1000) __builtin_amdgcn_s_sleep(1);
}
extern "C" void kernel_launch(void* const* d_in, const int* in_sizes, int n_in, void* d_out, int out_size, void* d_ws, size_t ws_size,
                              hipStream_t stream) {
  const u16* x = (const u16*)d_in[0];
  kpre<128,3><<<256,256,0,stream>>>((const char*)d_in[0],(const char*)d_in[1],(const char*)d_in[2],(const char*)d_in[3],(const char*)d_in[5],(const char*)d_in[6],(const char*)d_in[7],(const char*)d_ws,(const char*)d_out,(char*)d_ws);
  k1<<<256, 512, 0, stream>>>(x, (const u16*)d_in[1], (const u16*)d_in[2], (const u16*)d_in[5], (const u16*)d_in[6], (char*)d_ws);
  k2<<<256, 512, 0, stream>>>(x, (const u16*)d_in[3], (const u16*)d_in[7], (char*)d_ws, (u16*)d_out);
}
```

```cpp
#include <hip/hip_runtime.h>
#pragma clang diagnostic ignored "-Wint-to-pointer-cast"

#if defined(__HIP_DEVICE_COMPILE__)
#pragma clang attribute push(__attribute__((target("no-packed-fp32-ops"))), apply_to = function)
#endif
#define TID ((int)__builtin_amdgcn_workitem_id_x())
#define BID ((int)__builtin_amdgcn_workgroup_id_x())
#define ALL(c) (__builtin_amdgcn_ballot_w64(!(c)) == 0)
#define F2U(x) __builtin_bit_cast(u32, (float)(x))
#define U2F(x) __builtin_bit_cast(float, (u32)(x))

typedef unsigned short u16;
typedef unsigned int u32;
typedef unsigned long long u64;
typedef __attribute__((ext_vector_type(8))) __bf16 bf16x8;
typedef __attribute__((ext_vector_type(2))) __bf16 bf16x2;
typedef __attribute__((ext_vector_type(4))) short s16x4;
typedef __attribute__((ext_vector_type(16))) float f32x16;
typedef __attribute__((ext_vector_type(4))) float f32x4;
typedef __attribute__((ext_vector_type(4))) u32 u32x4;
typedef __attribute__((ext_vector_type(2))) u32 u32x2;
#define LAS __attribute__((address_space(3)))
#define GAS __attribute__((address_space(1)))
#define DI __device__ __forceinline__
#define SBAR() __builtin_amdgcn_sched_barrier(0)
#define PIN(x) asm volatile("" : "+v"(x))
#define RLX_AGENT __ATOMIC_RELAXED, __HIP_MEMORY_SCOPE_AGENT

constexpr int S_ = 2048, D_ = 448, F_ = 1792;
constexpr u32 WS_H = 0;
constexpr u32 WS_PQ = WS_H + 64 * 112 * 1024;
constexpr u32 WS_KVP = WS_PQ + 64 * 16 * 2048;
constexpr u32 WS_KVF = WS_KVP + 32 * 16 * 4096;
constexpr u32 WS_CS = WS_KVF + 65536;
constexpr u32 WS_ST = WS_CS + 16 * 10 * 2048;
constexpr u32 WS_FH1 = WS_ST + 2048 * 8;
constexpr u32 WS_FH2 = WS_FH1 + 256 * 128;
constexpr u32 WS_FKV = WS_FH2 + 256 * 128;
constexpr u32 WS_FR = WS_FKV + 512 * 128;
constexpr u32 WS_EP = WS_FR + 33 * 128;
constexpr u32 WS_END = WS_EP + 512;
constexpr u32 WS_DBG = WS_END;

#define STAMP(k) do {} while (0)
#define STAMPC(k) do {} while (0)

DI f32x16 mfma(u32x4 a, u32x4 b, f32x16 c) {
  return __builtin_amdgcn_mfma_f32_32x32x16_bf16(__builtin_bit_cast(bf16x8, a), __builtin_bit_cast(bf16x8, b), c, 0, 0, 0);
}
DI u32 pk2(float lo, float hi) { bf16x2 p; p[0] = (__bf16)lo; p[1] = (__bf16)hi; return __builtin_bit_cast(u32, p); }
DI float rbf(float f) { return (float)(__bf16)f; }
DI float lo16(u32 u) { return U2F(u << 16); }
DI float hi16(u32 u) { return U2F(u & 0xffff0000u); }
DI float dot2(u32 a, u32 b, float c) {
  return __builtin_amdgcn_fdot2_f32_bf16(__builtin_bit_cast(bf16x2, a), __builtin_bit_cast(bf16x2, b), c, false);
}
DI u32x2 ldtr(u32 addr) { return __builtin_bit_cast(u32x2, __builtin_amdgcn_ds_read_tr16_b64_v4i16((LAS s16x4*)addr)); }
DI u32x4 ldtr2(u32 a0, u32 a1) { u32x2 a = ldtr(a0), b = ldtr(a1); return u32x4{a[0], a[1], b[0], b[1]}; }
DI u32x4 ld128(u32 addr) { return *(LAS u32x4*)addr; }
DI void st128(u32 addr, u32x4 v) { *(LAS u32x4*)addr = v; }
DI void st64(u32 addr, u32x2 v) { *(LAS u32x2*)addr = v; }
DI u32x4 mksrd(const void* p, u32 bytes) {
  u64 a = (u64)p;
  return u32x4{(u32)a, (u32)(a >> 32) & 0xffffu, bytes, 0x00020000u};
}
DI void dma1(u32x4 srd, u32 v0, u32 soff, u32 lds) {
  u32 k;
  asm volatile("s_mov_b32 %0, m0\n\ts_mov_b32 m0, %4\n\ts_nop 0\n\tbuffer_load_dwordx4 %1, %2, %3 offen lds\n\ts_mov_b32 m0, %0"
               : "=&s"(k) : "v"(v0), "s"(srd), "s"(soff), "s"(lds) : "memory");
}
DI void dma2(u32x4 srd, u32 v0, u32 v1, u32 soff, u32 lds) {
  u32 k;
  asm volatile("s_mov_b32 %0, m0\n\ts_mov_b32 m0, %5\n\ts_nop 0\n\tbuffer_load_dwordx4 %1, %3, %4 offen lds\n\tbuffer_load_dwordx4 %2, %3, %4 offen offset:1024 lds\n\t"
               "s_mov_b32 m0, %0" : "=&s"(k) : "v"(v0), "v"(v1), "s"(srd), "s"(soff), "s"(lds) : "memory");
}
DI void dma4(u32x4 srd, u32 v0, u32 v1, u32 v2, u32 v3, u32 soff, u32 lds) {
  u32 k;
  asm volatile("s_mov_b32 %0, m0\n\ts_mov_b32 m0, %7\n\ts_nop 0\n\tbuffer_load_dwordx4 %1, %5, %6 offen lds\n\tbuffer_load_dwordx4 %2, %5, %6 offen offset:1024 lds\n\t"
               "buffer_load_dwordx4 %3, %5, %6 offen offset:2048 lds\n\tbuffer_load_dwordx4 %4, %5, %6 offen offset:3072 lds\n\ts_mov_b32 m0, %0"
               : "=&s"(k) : "v"(v0), "v"(v1), "v"(v2), "v"(v3), "s"(srd), "s"(soff), "s"(lds) : "memory");
}
#define WAITV(n) asm volatile("s_waitcnt vmcnt(%0)" ::"i"(n) : "memory")
#define BAR() do { asm volatile("" ::: "memory"); __builtin_amdgcn_s_waitcnt(0xC07F); __builtin_amdgcn_s_barrier(); asm volatile("" ::: "memory"); SBAR(); } while (0)
DI void xhalf(float v, float& lo, float& hi) {
  auto r = __builtin_amdgcn_permlane32_swap(F2U(v), F2U(v), false, false);
  lo = U2F(r[0]); hi = U2F(r[1]);
}
DI float xhalf_hi(float v) { float lo, hi; xhalf(v, lo, hi); return hi; }
constexpr int SPIN_CAP = 1 << 20;
DI void wait_flags(const char* p, u32 tag) {
#pragma unroll 1
  for (int spin = 0; spin < SPIN_CAP; ++spin) {
    const u32 v = __hip_atomic_load((GAS u32*)p, RLX_AGENT);
    if (ALL(v == tag)) break;
    __builtin_amdgcn_s_sleep(2);
  }
  __builtin_amdgcn_fence(__ATOMIC_ACQUIRE, "wavefront");
}
DI u32 lds_inc(u32 addr) {
  u32 o = 0;
  if ((TID & 63) == 0) o = __hip_atomic_fetch_add((LAS u32*)addr, 1u, __ATOMIC_RELAXED, __HIP_MEMORY_SCOPE_WORKGROUP);
  return __builtin_amdgcn_readfirstlane(o);
}
template <typename R> DI void st_wt(R rs, u32 off, u32x4 v) { __builtin_amdgcn_raw_buffer_store_b128(v, rs, off, 0, 16); }
DI void lds_wait(u32 addr, u32 n) {
#pragma unroll 1
  for (int spin = 0; spin < SPIN_CAP; ++spin)
    if (__builtin_amdgcn_readfirstlane(__hip_atomic_load((LAS u32*)addr, __ATOMIC_RELAXED, __HIP_MEMORY_SCOPE_WORKGROUP)) >= n) break;
  asm volatile("" ::: "memory");
}
DI void lds_sig(u32 addr) {
  asm volatile("" ::: "memory");
  *(volatile LAS u32*)addr = 1u;
  asm volatile("" ::: "memory");
}
DI void lds_wait4(u32 addr) {
#pragma unroll 1
  for (int spin = 0; spin < SPIN_CAP; ++spin) {
    const u32x4 v = *(volatile LAS u32x4*)addr;
    if (__builtin_amdgcn_readfirstlane(v[0] & v[1] & v[2] & v[3])) break;
  }
  asm volatile("" ::: "memory");
}
typedef __attribute__((ext_vector_type(2))) float f32x2;
DI float max3(float a, float b, float c) { float d; asm("v_max3_f32 %0, %1, %2, %3" : "=v"(d) : "v"(a), "v"(b), "v"(c)); return d; }
DI float min3(float a, float b, float c) { float d; asm("v_min3_f32 %0, %1, %2, %3" : "=v"(d) : "v"(a), "v"(b), "v"(c)); return d; }
DI float ex2(float a) { return __builtin_amdgcn_exp2f(a); }
#define TK(v)
constexpr float LOG2E = 1.4426950408889634f;

constexpr u32 L_SLOT = 40960, L_A = 8192, L_X = 24576;
constexpr u32 L_VF = 0, L_PHI = L_SLOT;
constexpr u32 L_R4 = 122880, L_WPQ = 139264, L_WPK = L_WPQ + 7168, L_FIX = L_WPK + 2048, L_ST = L_FIX + 10240, L_CTL = L_ST + 1024;
constexpr u32 C_A = L_CTL + 4, C_B = L_CTL + 8, C_R = L_CTL + 12, C_KV = L_CTL + 16, C_T1 = L_CTL + 32, C_T2 = L_CTL + 48, C_P1 = L_CTL + 64, C_P2 = L_CTL + 80;
DI constexpr u32 thome(int i) { return i == 0 ? L_R4 : (i < 3 ? L_SLOT + L_A + (i - 1) * 16384 : (i < 5 ? 2 * L_SLOT + L_A + (i - 3) * 16384 : L_A + (i - 5) * 16384)); }
constexpr int CODE_LINES = 256;

__global__ __launch_bounds__(512, 2) void fused(const u16* __restrict__ x, const u16* __restrict__ wqkv, const u16* __restrict__ wphi,
                                                const u16* __restrict__ wo, const u16* __restrict__ wg, const u16* __restrict__ wu,
                                                const u16* __restrict__ wd, char* __restrict__ ws, u16* __restrict__ out) {
  __shared__ __attribute__((aligned(16))) char smem[163840];
  u64 pc0; asm volatile("s_getpc_b64 %0" : "=s"(pc0));
  const u32 lb = (u32)(size_t)(LAS char*)smem;
  const int wid = __builtin_amdgcn_readfirstlane(TID >> 6);
  const int l = TID & 63;
  const int xcd = BID & 7, idx = BID >> 3;
  const int panel = (xcd >> 1) * 4 + (idx & 3);
  const int j = (xcd & 1) * 8 + (idx >> 2);
  const bool ytype = (j < 2) || (j >= 14);
  const int qg = j < 2 ? j : j - 12;
  const int line1 = 2 * j - 2;
  const int line2 = j < 2 ? j : (j >= 14 ? j + 12 : 2 * j - 1);
  const int koff = (j & 1) * 8;
  const int rot = (2 * panel + j) % 7;
  const int blk = (l >> 4) & 1, q = (l & 15) >> 2, p = l & 3, h = l >> 5, r = l & 31;
  const u32 rowpart = (8 * h + q) * 128, rx = 64 * ((q >> 1) & 1);
  const auto rsw = __builtin_amdgcn_make_buffer_rsrc((void*)ws, 0, WS_END, 0x00020000);
  auto ffn_addr = [&](int uu) { return lb + blk * 8192 + rowpart + ((32 * uu + 8 * p) ^ (32 * blk) ^ rx); };
  auto q_addr = [&](int c) { const u32 o = ((qg == 3 ? 32 : 0) + 56 * c + 32 * blk + 8 * p) & 255; return lb + (o >> 7) * 8192 + rowpart + ((o & 127) ^ rx); };
  const u32 k_addr = lb + rowpart + (((koff + 32 * blk + 8 * p) & 63) ^ rx);
  const u32 lc = (16 * (l & 7)) ^ (64 * ((l >> 4) & 1));
  auto xfix = [&](int t) { const u32 nm = *(LAS u32*)(lb + L_ST + 512 + (32 * t + r) * 4); return u32x4{nm, nm, nm, h ? 0u : nm}; };
  auto ffn = [&](const f32x16& a, float rs, int tile, int unit) {
    const float r2 = rs * rs;
    u32x4 o;
#pragma unroll
    for (int e = 0; e < 4; ++e) o[e] = pk2(__builtin_fmaxf(a[2 * e], 0.f) * a[8 + 2 * e] * r2, __builtin_fmaxf(a[2 * e + 1], 0.f) * a[9 + 2 * e] * r2);
    st_wt(rsw, l * 16 + WS_H + ((panel * 4 + tile) * 112 + unit) * 1024, o);
  };
#define MM(c, a, b) do { c = mfma(a, b, c); PIN(c); SBAR(); } while (0)
#define RW(Wr, a, no) do { Wr = ldtr2((a) + (no), (a) + (no) + 512); } while (0)
  const bool reducer = idx < 4;
  const int ti = xcd * 4 + ((idx - 4) & 3), ct = (idx - 4) >> 2;
  STAMP(0);

  if (wid >= 4) {
    const int w = wid - 4;
    const u32x4 srdQ = mksrd(wqkv, D_ * 3 * D_ * 2), srdG = mksrd(wg, D_ * F_ * 2), srdU = mksrd(wu, D_ * F_ * 2), srdX = mksrd(x, S_ * D_ * 2);
    const u32 vkv = (l >> 3) * 2688 + 896 + 56 * j - koff + (lc < 64 ? lc : lc + 896 - 64);
    const u32 vx0 = (l >> 3) * 896 + 16 * ((l & 7) ^ (l >> 4)), vx1 = (l >> 3) * 896 + 16 * ((l & 7) ^ (4 + (l >> 4))) + 6144;
    const u32 xrow0 = (panel * 128 + w * 32) * 896;
    const u32 st1 = ytype ? 2688 : 3584;
    const u32x4 srdA1 = ytype ? srdQ : srdG, srdB1 = ytype ? srdQ : srdU;
    const u32 va1 = (l >> 3) * st1 + (ytype ? (qg == 3 ? 640 : 224 * qg) : line1 * 128) + lc;
    const u32 vb1 = ytype ? va1 + 128 : (va1 ^ 32);
    const u32 va2 = (l >> 3) * 3584 + line2 * 128 + lc, vb2 = va2 ^ 32;
    auto kblk = [&](int i) { const int kb = rot + i; return kb >= 7 ? kb - 7 : kb; };
    auto issueS = [&](int i) {
      const int kb = kblk(i); const u32 slot = lb + (i % 3) * L_SLOT;
      dma4(srdX, vx0, vx1, vx0 + 2 * 6144, vx1 + 2 * 6144, xrow0 + kb * 128, slot + L_X + w * 4096);
      dma2(srdA1, va1, va1 + 8 * st1 - 1024, (64 * kb + 16 * w) * st1, slot + L_A + w * 2048);
      dma2(srdB1, vb1, vb1 + 8 * st1 - 1024, (64 * kb + 16 * w) * st1, slot + L_A + 8192 + w * 2048);
      dma2(srdQ, vkv, vkv + 8 * 2688 - 1024, (64 * kb + 16 * w) * 2688, slot + w * 2048);
    };
    auto issueT = [&](int i) {
      const int kb = kblk(i);
      dma2(srdG, va2, va2 + 8 * 3584 - 1024, (64 * kb + 16 * w) * 3584, lb + thome(i) + w * 2048);
      dma2(srdU, vb2, vb2 + 8 * 3584 - 1024, (64 * kb + 16 * w) * 3584, lb + thome(i) + 8192 + w * 2048);
    };
    issueS(0); issueS(1);
    const bool prod = panel < 7;
    const u32 csb = WS_CS + j * 20480;
    u32 tag;
    auto rd4 = [&](u32x4 (&f)[4], u32 a) {
#pragma unroll
      for (int k = 0; k < 4; ++k) f[k] = ldtr2(a + k * 2048, a + k * 2048 + 512);
    };
    auto pubcs = [&](const u32x4 (&f)[4], int unit) {
      const u32x4 ones = {0x3f803f80u, 0x3f803f80u, 0x3f803f80u, 0x3f803f80u};
      f32x16 c = {};
#pragma unroll
      for (int k = 0; k < 4; ++k) c = mfma(ones, f[k], c);
      const float s = c[0], s0 = rbf(s);
      if (l < 32) __builtin_amdgcn_raw_buffer_store_b64(u32x2{pk2(s0, s - s0), tag}, rsw, r * 8, csb + unit * 2048 + rot * 256, 16);
    };
    const u32 goff = h * 1024 + r * 8;
    const u32x4 srdS = mksrd(ws, WS_END);
    auto gather = [&](u32x2 (&g)[4], int unit) {
      asm volatile("buffer_load_dwordx2 %0, %4, %6, %7 offen sc1\n\tbuffer_load_dwordx2 %1, %4, %6, %7 offen offset:256 sc1\n\t"
                   "buffer_load_dwordx2 %2, %4, %6, %7 offen offset:512 sc1\n\tbuffer_load_dwordx2 %3, %5, %6, %7 offen offset:768 sc1"
                   : "=&v"(g[0]), "=&v"(g[1]), "=&v"(g[2]), "=&v"(g[3]) : "v"(goff), "v"(goff - h * 256), "s"(srdS), "s"(csb + unit * 2048) : "memory");
    };
    u32x4 X[7][4];
    const u32 ax = lb + L_X + (32 * w + r) * 128 + 16 * (h ^ ((r >> 1) & 7));
    auto keepx = [&](int i) {
#pragma unroll
      for (int k = 0; k < 4; ++k) X[i][k] = ld128((ax ^ (32 * k)) + (i % 3) * L_SLOT);
    };
    u32x4 f0[4], f1[4];
    TK(e0); WAITV(10); BAR();
    TK(k0);
    keepx(0);
    if (prod) { rd4(f0, (ytype ? q_addr(w) : ffn_addr(w)) + L_A); rd4(f1, k_addr ^ (64 * (w & 1))); }
    issueT(0); issueS(2);
    tag = __builtin_amdgcn_readfirstlane(*(LAS u32*)(lb + L_CTL));
    if (prod) pubcs(f0, w);
    TK(e1); WAITV(10); BAR();
    TK(k1);
    keepx(1);
    if (prod) rd4(f0, ffn_addr(w) + thome(0));
    issueS(3);
    if (prod) { if (w < 2) pubcs(f1, 4 + w); pubcs(f0, 6 + w); }
    TK(e2); WAITV(10); BAR();
    TK(k2);
    keepx(2); issueS(4);
    TK(e3); WAITV(10); BAR();
    TK(k3);
    keepx(3); issueS(5);
    TK(e4); WAITV(10); BAR();
    TK(k4);
    keepx(4); issueS(6);
    u32x2 g0[4], g1[4], g2[4], s2;
    auto gather_all = [&]() {
      gather(g0, w); gather(g1, 4 + (w & 1)); gather(g2, 6 + w);
      asm volatile("buffer_load_dwordx2 %0, %1, %2, %3 offen sc1" : "=&v"(s2) : "v"(r * 8), "s"(srdS), "s"(WS_ST + (panel * 128 + w * 32) * 8) : "memory");
    };
#define WAITG(n) asm volatile("s_waitcnt vmcnt(%13)" : "+v"(g0[0]), "+v"(g0[1]), "+v"(g0[2]), "+v"(g0[3]), "+v"(g1[0]), "+v"(g1[1]), "+v"(g1[2]), "+v"(g1[3]), \
                              "+v"(g2[0]), "+v"(g2[1]), "+v"(g2[2]), "+v"(g2[3]), "+v"(s2) : "i"(n) : "memory")
    auto gbad = [&]() {
      u32 bad = s2[1] ^ tag;
#pragma unroll
      for (int i = 0; i < 4; ++i) bad |= (g0[i][1] ^ tag) | (g1[i][1] ^ tag) | (g2[i][1] ^ tag);
      return !ALL(bad == 0u);
    };
    WAITV(10); BAR();
    TK(k5);
    keepx(5); gather_all(); issueT(1);
    TK(e6); WAITV(13 + 4); BAR();
    TK(k6);
    keepx(6); issueT(2); issueT(3);
    WAITG(12);
#pragma unroll 1
    for (int spin = 0; spin < SPIN_CAP && gbad(); ++spin) { __builtin_amdgcn_s_sleep(1); gather_all(); WAITG(0); }
    st128(lb + L_FIX + w * 1024 + l * 16, u32x4{g0[0][0], g0[1][0], g0[2][0], g0[3][0]});
    st128(lb + L_FIX + 4096 + (w & 1) * 1024 + l * 16, u32x4{g1[0][0], g1[1][0], g1[2][0], g1[3][0]});
    st128(lb + L_FIX + 6144 + w * 1024 + l * 16, u32x4{g2[0][0], g2[1][0], g2[2][0], g2[3][0]});
    *(LAS u32*)(lb + L_ST + h * 512 + (32 * w + r) * 4) = h ? ((s2[0] & 0xffffu) ^ 0x8000u) * 0x10001u : F2U(__builtin_amdgcn_rcpf(hi16(s2[0])));
    TK(e7); WAITV(8); BAR();
    TK(k7);
    issueT(4); issueT(5); issueT(6);
#define BPRIO 2
    __builtin_amdgcn_s_setprio(BPRIO);
    STAMP(2); STAMPC(14);
    f32x16 c0 = {}, c1 = {}, c2 = {}, c3 = {};
    u32x4 W0, W1, W2, W3, W0n, W1n, W2n, W3n;
    u32 b0 = ffn_addr(0) + thome(0), b1 = ffn_addr(1) + thome(0), b2 = ffn_addr(2) + thome(0), b3 = ffn_addr(3) + thome(0);
    PIN(b0); PIN(b1); PIN(b2); PIN(b3);
    RW(W0, b0, 0u); RW(W1, b1, 0u); RW(W2, b2, 0u); RW(W3, b3, 0u);
    u32 fl = 0;
    bool early = false;
    u32x4 pq[4][2][2];
    auto loadpq = [&]() {
      __builtin_amdgcn_fence(__ATOMIC_ACQUIRE, "wavefront");
#pragma unroll
      for (int c = 0; c < 4; ++c)
#pragma unroll
        for (int b = 0; b < 2; ++b)
#pragma unroll
          for (int s = 0; s < 2; ++s)
            pq[c][b][s] = __builtin_amdgcn_raw_buffer_load_b128(rsw, l * 16, WS_PQ + ((2 * ti + b) * 16 + 4 * w + c) * 2048 + s * 1024, 16);
    };
    const u32 fx = lb + L_FIX + 6144 + l * 16;
#pragma unroll
    for (int i = 0; i < 7; ++i) {
#pragma unroll
      for (int k = 0; k < 4; ++k) {
        if (k == 0 && i == 1) { WAITV(12); lds_sig(lb + C_T1 + 4 * w); }
        if (k == 0 && i == 3) { WAITV(0); lds_sig(lb + C_T2 + 4 * w); }
        if (k == 1 && i == 3) lds_sig(lb + C_P1 + 4 * w);
        if (k == 1 && i == 5) lds_sig(lb + C_P2 + 4 * w);
        if (k == 0 && i == 5 && !reducer) fl = __hip_atomic_load((GAS u32*)(ws + WS_FH1 + ((ti >> 1) * 16 + (w < 2 ? w : w + 12)) * 128), RLX_AGENT);
        if (k == 1 && i == 6 && !reducer && __builtin_amdgcn_readfirstlane(fl) == tag) { early = true; loadpq(); }
        if (k == 3 && i < 6) {
          if (i == 1) lds_wait4(lb + C_T1);
          if (i == 3) lds_wait4(lb + C_T2);
          const u32 d = thome(i + 1) - thome(i);
          b0 += d; b1 += d; b2 += d; b3 += d; PIN(b0); PIN(b1); PIN(b2); PIN(b3);
        }
        const u32 no = k == 3 ? 0u : (u32)(k + 1) * 2048u;
        const bool last = i == 6 && k == 3;
        SBAR();
        if (last) W0n = ld128(fx); else RW(W0n, b0, no);
        SBAR(); MM(c0, W0, X[i][k]);
        if (last) W1n = ld128(fx + 1024); else RW(W1n, b1, no);
        SBAR(); MM(c1, W1, X[i][k]);
        if (last) W2n = ld128(fx + 2048); else RW(W2n, b2, no);
        SBAR(); MM(c2, W2, X[i][k]);
        if (last) W3n = ld128(fx + 3072); else RW(W3n, b3, no);
        SBAR(); MM(c3, W3, X[i][k]);
        W0 = W0n; W1 = W1n; W2 = W2n; W3 = W3n;
      }
    }
    const u32x4 M0 = xfix(w);
    const float rs0 = *(LAS float*)(lb + L_ST + (32 * w + r) * 4);
    MM(c0, W0, M0); MM(c1, W1, M0); MM(c2, W2, M0); MM(c3, W3, M0);
    STAMP(3); STAMPC(15);
    ffn(c0, rs0, w, line2 * 4); ffn(c1, rs0, w, line2 * 4 + 1); ffn(c2, rs0, w, line2 * 4 + 2); ffn(c3, rs0, w, line2 * 4 + 3);
    WAITV(0);
    if (lds_inc(lb + C_B) == 3u && l == 0) __hip_atomic_store((GAS u32*)(ws + WS_FH2 + (panel * 16 + j) * 128), tag, RLX_AGENT);
    STAMP(4);
    if (reducer) return;
    if (!early) { wait_flags(ws + WS_FH1 + ((ti >> 1) * 16 + (w < 2 ? w : w + 12)) * 128, tag); loadpq(); }
    u32x4 ef[4][2][4];
    auto soft = [&](int c, int b) {
      float mx, mn, t_;
      xhalf(U2F(pq[c][b][1][2]), t_, mx);
      mn = hi16(F2U(mx)) * LOG2E; mx = lo16(F2U(mx)) * -LOG2E;
#pragma unroll
      for (int e = 0; e < 8; ++e) {
        const u32 u = pq[c][b][e >> 2][e & 3];
        const f32x2 v = {lo16(u), hi16(u)};
        const f32x2 ap = v * LOG2E + mx, an = v * -LOG2E + mn;
        ef[c][b][e >> 2][e & 3] = pk2(ex2(ap[0]), ex2(ap[1])); ef[c][b][2 + (e >> 2)][e & 3] = pk2(ex2(an[0]), ex2(an[1]));
      }
#pragma unroll
      for (int s = 0; s < 4; ++s) PIN(ef[c][b][s]);
      SBAR();
    };
    soft(0, 0); soft(0, 1);
    STAMP(5);
    soft(1, 0); soft(1, 1); soft(2, 0); soft(2, 1);
    STAMP(6);
    wait_flags(ws + WS_FR + (8 * w + (l & 7)) * 128, tag);
    STAMP(7);
    u32x4 kv[4][4];
#pragma unroll
    for (int c = 0; c < 4; ++c)
#pragma unroll
      for (int s = 0; s < 4; ++s) kv[c][s] = __builtin_amdgcn_raw_buffer_load_b128(rsw, l * 16, WS_KVF + (4 * w + c) * 4096 + s * 1024, 16);
    SBAR();
    soft(3, 0); soft(3, 1);
    lds_wait(lb + C_B, 4u); lds_wait4(lb + C_KV);
    STAMP(8);
    const u32 ob = lb + (w < 3 ? w * 16384 : L_WPQ) + l * 16;
#pragma unroll
    for (int c = 0; c < 4; ++c)
#pragma unroll
      for (int b = 0; b < 2; ++b) {
        f32x16 ap = {}, an = {};
        ap = mfma(kv[c][0], ef[c][b][0], ap); an = mfma(kv[c][2], ef[c][b][2], an);
        ap = mfma(kv[c][1], ef[c][b][1], ap); an = mfma(kv[c][3], ef[c][b][3], an);
        const float rp = __builtin_amdgcn_rcpf(ap[13]), rn = __builtin_amdgcn_rcpf(an[13]);
        const float rd = __builtin_amdgcn_rcpf(ap[12] * rp + an[12] * rn + 1e-6f);
        const float ca = xhalf_hi(rp * rd), cb = xhalf_hi(rn * rd);
        u32x4 o0, o1;
#pragma unroll
        for (int e = 0; e < 4; ++e) {
          o0[e] = pk2(ap[2 * e] * ca + an[2 * e] * cb, ap[2 * e + 1] * ca + an[2 * e + 1] * cb);
          o1[e] = pk2(ap[8 + 2 * e] * ca + an[8 + 2 * e] * cb, ap[9 + 2 * e] * ca + an[9 + 2 * e] * cb);
        }
        o1[2] = h ? 0u : o1[2]; o1[3] = h ? 0u : o1[3];
        st128(ob + c * 4096 + b * 2048, o0); st128(ob + c * 4096 + b * 2048 + 1024, o1);
      }
    STAMP(9);
    BAR();
    return;
  }

  const int m = wid >> 1, n = wid & 1;
  const int t0 = 2 * m + n, t1 = 2 * m + 1 - n;
  if (wid == 0) {
    const u32 ep = __hip_atomic_load((GAS u32*)(ws + WS_EP), RLX_AGENT);
    const u32x4* src = (const u32x4*)(x + (panel * 128 + j * 8 + (l >> 3)) * D_) + (l & 7);
    u32x4 xs[7];
#pragma unroll
    for (int i = 0; i < 7; ++i) xs[i] = src[i * 8];
    const u32 tag0 = __builtin_amdgcn_readfirstlane(ep) * 1664525u + 1013904223u;
    if (l < 32) *(LAS u32*)(lb + L_CTL + 4 * l) = l == 0 ? tag0 : 0u;
    float sx = 0.f, sq = 0.f;
#pragma unroll
    for (int i = 0; i < 7; ++i)
#pragma unroll
      for (int e = 0; e < 4; ++e) { sx = dot2(xs[i][e], 0x3f803f80u, sx); sq = dot2(xs[i][e], xs[i][e], sq); }
#pragma unroll
    for (int o = 1; o < 8; o <<= 1) { sx += U2F(__builtin_amdgcn_ds_bpermute((l ^ o) * 4, F2U(sx))); sq += U2F(__builtin_amdgcn_ds_bpermute((l ^ o) * 4, F2U(sq))); }
    const float mean = sx * (1.f / D_);
    const float var = __builtin_fmaxf(sq * (1.f / D_) - mean * mean, 0.f);
    const float sd = __builtin_amdgcn_sqrtf(rbf(rbf(var) + 1e-5f));
    if ((l & 7) == 0) __hip_atomic_store((GAS u64*)(ws + WS_ST + (panel * 128 + j * 8 + (l >> 3)) * 8), (u64)pk2(mean, sd) | ((u64)tag0 << 32), RLX_AGENT);
  } else if (wid == 1) {
    const u32x4 srdP = mksrd(wphi, 16 * 784 * 2);
    dma2(srdP, l * 16, l * 16, j * 1568, lb + L_WPK);
    if (ytype) { dma4(srdP, l * 16, l * 16, l * 16, l * 16, qg * 6272, lb + L_WPQ); dma2(srdP, l * 16, l * 16, qg * 6272 + 4096, lb + L_WPQ + 4096); dma1(srdP, l * 16, qg * 6272 + 6144, lb + L_WPQ + 6144); }
    WAITV(0);
  }
  else if (wid == 2) {
    const int cl = idx * 8 + (l & 7);
    u32 t = 0;
    if (l < 8 && cl < CODE_LINES) t = *(const volatile u32*)((pc0 & ~127ull) + cl * 128);
    const int wl = BID * 64 + l;
    if (wl < 12544) t += *(const volatile u32*)((const char*)wd + wl * 128);
    else if (wl < 15680) t += *(const volatile u32*)((const char*)wo + (wl - 12544) * 128);
    asm volatile("" ::"v"(t));
  }
  u32 a0 = (ytype ? q_addr(2 * n) : ffn_addr(2 * n)) + L_A, a1 = (ytype ? q_addr(2 * n + 1) : ffn_addr(2 * n + 1)) + L_A;
  u32 ak = k_addr, av = k_addr ^ 64;
  u32 ax0 = lb + L_X + (32 * t0 + r) * 128 + 16 * (h ^ ((r >> 1) & 7));
  u32 ax1 = ax0 ^ 4096;
  f32x16 c00 = {}, c01 = {}, c10 = {}, c11 = {}, ck = {}, cv = {};
  u32x4 X0 = {}, X1 = {}, W0 = {}, W1 = {}, WK = {}, WV = {}, X0n, X1n, W0n, W1n, WKn, WVn;
#define XSTEP(xx) do { ax0 ^= (xx); ax1 ^= (xx); PIN(ax0); PIN(ax1); } while (0)
#define REST1(no) do { RW(W0n, a0, no); X0n = ld128(ax0); SBAR(); MM(c01, W0, X1); X1n = ld128(ax1); RW(W1n, a1, no); SBAR(); MM(c10, W1, X0); \
    RW(WKn, ak, no); SBAR(); MM(c11, W1, X1); RW(WVn, av, no); SBAR(); MM(ck, WK, X0); MM(cv, X0, WV); \
    X0 = X0n; X1 = X1n; W0 = W0n; W1 = W1n; WK = WKn; WV = WVn; } while (0)
  PIN(c00); PIN(c01); PIN(c10); PIN(c11); PIN(ck); PIN(cv); PIN(X0); PIN(X1); PIN(W0); PIN(W1); PIN(WK); PIN(WV);
  BAR();
  STAMP(1); STAMPC(14);
  int ph = 0;
#pragma unroll 1
  for (int it = 0; it < 7; ++it) {
    REST1(0u);
    XSTEP(32u); MM(c00, W0, X0); REST1(2048u);
    XSTEP(96u); MM(c00, W0, X0); REST1(4096u);
    XSTEP(32u); MM(c00, W0, X0); REST1(6144u);
    const u32 delta = ph == 2 ? 0u - 2 * L_SLOT : L_SLOT; ph = ph == 2 ? 0 : ph + 1;
    ax0 = (ax0 ^ 96u) + delta; ax1 = (ax1 ^ 96u) + delta; a0 += delta; a1 += delta; ak += delta; av += delta;
    PIN(ax0); PIN(ax1); PIN(a0); PIN(a1); PIN(ak); PIN(av);
    SBAR();
    MM(c00, W0, X0); BAR();
  }
  const u32 fx = lb + L_FIX + l * 16;
  WKn = ld128(fx + 4096); WVn = ld128(fx + 5120); W0n = ld128(fx + n * 2048); W1n = ld128(fx + n * 2048 + 1024);
  const u32x4 M0 = xfix(t0), M1 = xfix(t1);
  const float rs0 = *(LAS float*)(lb + L_ST + (32 * t0 + r) * 4), rs1 = *(LAS float*)(lb + L_ST + (32 * t1 + r) * 4);
  const u32 tag = __builtin_amdgcn_readfirstlane(*(LAS u32*)(lb + L_CTL));
  SBAR();
  MM(c01, W0, X1); MM(c10, W1, X0); MM(c11, W1, X1); MM(ck, WK, X0); MM(cv, X0, WV);
  STAMP(2); STAMPC(15);
  MM(c00, W0n, M0); MM(c01, W0n, M1); MM(c10, W1n, M0); MM(c11, W1n, M1); MM(ck, WKn, M0); MM(cv, M0, WVn);
  const u32 wpl = (4 * h + q) * 56 + 32 * blk + 8 * p;
  auto prephi = [&](const f32x16& a, float rs, u32 wp, float& mx, float& mn) {
    u32x4 b0, b1;
#pragma unroll
    for (int e = 0; e < 4; ++e) { b0[e] = pk2(a[2 * e] * rs, a[2 * e + 1] * rs); b1[e] = pk2(a[8 + 2 * e] * rs, a[9 + 2 * e] * rs); }
    b1[2] = h ? 0u : b1[2]; b1[3] = h ? 0u : b1[3];
    f32x16 pz = {};
    pz = mfma(ldtr2(wp, wp + 8 * 56), b0, pz);
    pz = mfma(ldtr2(wp + 16 * 56, wp + 24 * 56), b1, pz);
    float x1 = __builtin_fmaxf(pz[0], pz[1]), n1 = __builtin_fminf(pz[0], pz[1]), x2 = __builtin_fmaxf(pz[12], pz[13]), n2 = __builtin_fminf(pz[12], pz[13]);
#pragma unroll
    for (int e = 2; e < 12; e += 2) { x1 = max3(x1, pz[e], pz[e + 1]); n1 = min3(n1, pz[e], pz[e + 1]); }
    x2 = max3(x2, pz[14], pz[15]); n2 = min3(n2, pz[14], pz[15]);
    float lo, hi, lo2, hi2;
    xhalf(x1, lo, hi); xhalf(x2, lo2, hi2); mx = max3(lo, hi, lo2);
    xhalf(n1, lo, hi); xhalf(n2, lo2, hi2); mn = min3(lo, hi, lo2);
    return pz;
  };
  if (!ytype) {
    const int ub = line1 * 4 + 2 * n;
    ffn(c00, rs0, t0, ub); ffn(c01, rs1, t1, ub); ffn(c10, rs0, t0, ub + 1); ffn(c11, rs1, t1, ub + 1);
  } else {
    auto qout = [&](const f32x16& a, float rs, int tile, int c) {
      float mx, mn;
      const f32x16 z = prephi(a, rs, lb + L_WPQ + c * 1568 + wpl, mx, mn);
      u32x4 o0, o1;
#pragma unroll
      for (int e = 0; e < 4; ++e) { o0[e] = pk2(z[2 * e], z[2 * e + 1]); o1[e] = pk2(z[8 + 2 * e], z[9 + 2 * e]); }
      const u32 mm = pk2(mx, mn);
      o1[2] = h ? mm : o1[2]; o1[3] = h ? mm : o1[3];
      const u32 so = l * 16 + WS_PQ + ((panel * 4 + tile) * 16 + qg * 4 + c) * 2048;
      st_wt(rsw, so, o0); st_wt(rsw, so + 1024, o1);
    };
    qout(c00, rs0, t0, 2 * n); qout(c01, rs1, t1, 2 * n); qout(c10, rs0, t0, 2 * n + 1); qout(c11, rs1, t1, 2 * n + 1);
  }
  STAMP(3);
  {
    float mx, mn;
    f32x16 pz = prephi(ck, rs0, lb + L_WPK + wpl, mx, mn);
    mx *= -LOG2E; mn *= LOG2E;
    f32x2 ep[8], en[8], sp = {}, sn = {};
#pragma unroll
    for (int e = 0; e < 8; ++e) {
      const f32x2 v = {pz[2 * e], pz[2 * e + 1]};
      const f32x2 ap = v * LOG2E + mx, an = v * -LOG2E + mn;
      ep[e] = f32x2{ex2(ap[0]), ex2(ap[1])}; en[e] = f32x2{ex2(an[0]), ex2(an[1])};
      if (e >= 6) { ep[e] = h ? f32x2{} : ep[e]; en[e] = h ? f32x2{} : en[e]; }
      sp += ep[e]; sn += en[e];
    }
    float lo, hi;
    xhalf(sp[0] + sp[1], lo, hi); const float ip = __builtin_amdgcn_rcpf(lo + hi);
    xhalf(sn[0] + sn[1], lo, hi); const float in = __builtin_amdgcn_rcpf(lo + hi);
    const u32 fr = (r & 5) | ((r & 2) << 2) | ((r & 8) >> 2);
    const u32 pb = lb + L_PHI + m * L_SLOT + (32 * n + r) * 128;
#pragma unroll
    for (int g = 0; g < 4; ++g) {
      const f32x2 p0 = ep[2 * g] * ip, p1 = ep[2 * g + 1] * ip, n0 = en[2 * g] * in, n1 = en[2 * g + 1] * in;
      st64(pb + 8 * ((2 * g + h) ^ fr), u32x2{pk2(p0[0], p0[1]), pk2(p1[0], p1[1])});
      st64(pb + 8 * ((8 + 2 * g + h) ^ fr), u32x2{pk2(n0[0], n0[1]), pk2(n1[0], n1[1])});
    }
    u32x4 vb[2];
#pragma unroll
    for (int g = 0; g < 4; ++g) {
      const f32x4 rv = *(LAS f32x4*)(lb + L_ST + (32 * t0 + 8 * g + 4 * h) * 4);
      u32 a = pk2(cv[4 * g] * rv[0], cv[4 * g + 1] * rv[1]), b = pk2(cv[4 * g + 2] * rv[2], cv[4 * g + 3] * rv[3]);
      const u32 alt = r == 28 ? 0x3f803f80u : 0u;
      vb[g >> 1][2 * (g & 1)] = r < 28 ? a : alt; vb[g >> 1][2 * (g & 1) + 1] = r < 28 ? b : alt;
    }
    st128(lb + L_VF + t0 * 2048 + l * 16, vb[0]); st128(lb + L_VF + t0 * 2048 + 1024 + l * 16, vb[1]);
  }
  asm volatile("s_waitcnt vmcnt(0) lgkmcnt(0)" ::: "memory");
  if (lds_inc(lb + C_A) == 3u && l == 0) __hip_atomic_store((GAS u32*)(ws + WS_FH1 + (panel * 16 + j) * 128), tag, RLX_AGENT);
  STAMP(4);
  const int w = wid;
  if (w < 2) {
    lds_wait(lb + C_A, 4u);
    const u32 fq = ((q >> 1) << 3) | (h << 2) | (q & 1);
    const u32 sl = 8 * w + 4 * blk + p;
    const u32 b0 = lb + L_PHI + (4 * h + q) * 128 + 8 * (sl ^ fq);
    const u32 b1 = lb + L_PHI + (8 + 4 * h + q) * 128 + 8 * (sl ^ fq ^ 2);
    f32x16 acc = {};
#pragma unroll
    for (int s = 0; s < 8; ++s) {
      const u32 o = (s & 3) * 2048 + (s >> 2) * L_SLOT;
      acc = mfma(ldtr2(b0 + o, b1 + o), ld128(lb + L_VF + s * 1024 + l * 16), acc);
    }
#pragma unroll
    for (int g = 0; g < 4; ++g)
      st_wt(rsw, l * 16 + WS_KVP + ((j * 2 + w) * 16 + panel) * 4096 + g * 1024, __builtin_bit_cast(u32x4, f32x4{acc[4 * g], acc[4 * g + 1], acc[4 * g + 2], acc[4 * g + 3]}));
    lds_sig(lb + C_KV + 4 * w); lds_sig(lb + C_KV + 8 + 4 * w);
    WAITV(0);
    if (l == 0) __hip_atomic_store((GAS u32*)(ws + WS_FKV + ((j * 2 + w) * 16 + panel) * 128), tag, RLX_AGENT);
  }
  STAMP(5);
  if (reducer) {
    const int hf = xcd * 4 + idx;
    wait_flags(ws + WS_FKV + (hf * 16 + (l & 15)) * 128, tag);
    STAMP(6);
    f32x4 v[16];
#pragma unroll
    for (int i = 0; i < 16; ++i) v[i] = __builtin_bit_cast(f32x4, __builtin_amdgcn_raw_buffer_load_b128(rsw, l * 16, WS_KVP + (hf * 16 + i) * 4096 + w * 1024, 16));
#pragma unroll
    for (int o = 8; o; o >>= 1)
#pragma unroll
      for (int i = 0; i < o; ++i) v[i] += v[i + o];
    u64 val = (u64)pk2(v[0][0], v[0][1]) | ((u64)pk2(v[0][2], v[0][3]) << 32);
    if (r == 29) val = (w == 3 && h) ? 0ull : 0x3f803f803f803f80ull;
    __hip_atomic_store((GAS u64*)(ws + WS_KVF + hf * 2048 + (w >> 1) * 1024 + l * 16 + (w & 1) * 8), val, RLX_AGENT);
    WAITV(0);
    if (lds_inc(lb + C_R) == 3u && l == 0) __hip_atomic_store((GAS u32*)(ws + WS_FR + hf * 128), tag, RLX_AGENT);
    STAMP(7);
    return;
  }
  const u32x4 srdD = mksrd(wd, F_ * D_ * 2), srdO = mksrd(wo, D_ * D_ * 2);
  const u32 vw0 = (l >> 3) * 896 + ct * 128 + lc;
  const int rotE = (ct + 3 * (ti & 1)) % 6, rotL = (ct + 2 * (ti & 3)) & 7;
  auto blk2 = [&](int z) {
    if (z < 6) { int e = z + rotE; e = e >= 6 ? e - 6 : e; return 2 * (2 + 6 * w + 2 * (e >> 1)) + (e & 1); }
    const int e = (z - 6 + rotL) & 7;
    return 2 * (e < 6 ? 3 + 6 * w + 2 * (e >> 1) : (w < 2 ? w : w + 24)) + (e & 1);
  };
  auto issueW2 = [&](int z) {
    const u32 base = lb + thome(z % 5) + w * 4096;
    if (z < 14) dma4(srdD, vw0, vw0 + 6144, vw0 + 2 * 6144, vw0 + 3 * 6144, blk2(z) * (32 * 896), base);
    else dma4(srdO, vw0, vw0 + 6144, vw0 + 2 * 6144, vw0 + 3 * 6144, (4 * w + z - 14) * (28 * 896), base);
  };
  lds_wait4(lb + C_P1);
  issueW2(0); issueW2(1); issueW2(2);
  wait_flags(ws + WS_FH1 + ((ti >> 1) * 16 + 2 + 3 * w + ((l & 3) < 2 ? (l & 3) : 2)) * 128, tag);
  STAMP(6);
  const u32x4 srdH = mksrd(ws + WS_H, 64 * 112 * 1024);
  const u32 voffH = l * 16;
  u32x4 hb[14][4];
  auto issueH = [&](int z) {
    const u32 s0 = (2 * ti * 112 + 2 * blk2(z)) * 1024, s1 = s0 + 112 * 1024;
    asm volatile("buffer_load_dwordx4 %0, %4, %5, %6 offen sc1\n\tbuffer_load_dwordx4 %1, %4, %5, %6 offen offset:1024 sc1\n\t"
                 "buffer_load_dwordx4 %2, %4, %5, %7 offen sc1\n\tbuffer_load_dwordx4 %3, %4, %5, %7 offen offset:1024 sc1"
                 : "=&v"(hb[z][0]), "=&v"(hb[z][1]), "=&v"(hb[z][2]), "=&v"(hb[z][3]) : "v"(voffH), "s"(srdH), "s"(s0), "s"(s1) : "memory");
  };
#pragma unroll
  for (int z = 0; z < 6; ++z) issueH(z);
  lds_wait4(lb + C_P2);
  issueW2(3); issueW2(4);
  const u32 aw0 = lb + w * 4096 + (4 * h + q) * 128 + ((32 * blk + 8 * p) ^ rx);
  f32x16 acc[2][2] = {};
  auto stage = [&](int z, const u32x4 (&B)[4]) {
    u32x4 A[2][2];
    u32 s0 = aw0 + thome(z % 5); PIN(s0);
    u32 s1 = s0 ^ 64; PIN(s1);
#pragma unroll
    for (int s = 0; s < 2; ++s) {
      A[0][s] = ldtr2(s0 + s * 2048, s0 + s * 2048 + 1024);
      A[1][s] = ldtr2(s1 + s * 2048, s1 + s * 2048 + 1024);
    }
    if (z + 5 < 18) {
      asm volatile("s_waitcnt lgkmcnt(0)" : "+v"(A[0][0]), "+v"(A[0][1]), "+v"(A[1][0]), "+v"(A[1][1])::"memory");
      issueW2(z + 5);
    }
#pragma unroll
    for (int s = 0; s < 2; ++s)
#pragma unroll
      for (int a = 0; a < 2; ++a)
#pragma unroll
        for (int b = 0; b < 2; ++b) { acc[a][b] = mfma(A[a][s], B[2 * b + s], acc[a][b]); PIN(acc[a][b]); }
    SBAR();
  };
#define WAITH(z, n) asm volatile("s_waitcnt vmcnt(%4)" : "+v"(hb[z][0]), "+v"(hb[z][1]), "+v"(hb[z][2]), "+v"(hb[z][3]) : "i"(n) : "memory")
  WAITH(0, 28); stage(0, hb[0]);
  WAITH(1, 28); stage(1, hb[1]);
  WAITH(2, 28); stage(2, hb[2]);
  WAITH(3, 16); stage(3, hb[3]);
  const char* f2 = ws + WS_FH2 + ((ti >> 1) * 16 + ((l & 3) < 3 ? 2 + 3 * w + (l & 3) : (w < 2 ? w : w + 12))) * 128;
  u32 fl2;
  asm volatile("global_load_dword %0, %1, off sc1" : "=&v"(fl2) : "v"(f2) : "memory");
  WAITH(4, 17); stage(4, hb[4]);
  asm volatile("s_waitcnt vmcnt(4)" : "+v"(fl2)::"memory");
  if (!ALL(fl2 == tag)) wait_flags(f2, tag);
  STAMP(7);
#pragma unroll
  for (int z = 6; z < 14; ++z) issueH(z);
  WAITH(5, 36); stage(5, hb[5]);
#pragma unroll
  for (int z = 6; z < 10; ++z) { WAITH(z, 32); stage(z, hb[z]); }
#pragma unroll
  for (int z = 10; z < 14; ++z) { WAITH(z, 16); stage(z, hb[z]); }
  STAMP(8);
  int tz = TID; PIN(tz);
  const size_t orow = (size_t)(ti * 64 + (w >> 1) * 32 + (tz & 31)) * D_ + ct * 64 + (w & 1) * 32 + ((tz >> 3) & 4);
  u32x2 xr[4];
#pragma unroll
  for (int g = 0; g < 4; ++g) xr[g] = *(const u32x2*)(x + orow + 8 * g);
  u32 ao = lb + (w < 3 ? w * 16384 : L_WPQ) + (tz & 63) * 16; PIN(ao);
  BAR();
  STAMP(9);
#pragma unroll
  for (int z = 14; z < 18; ++z) {
    WAITV((17 - z) * 4 + 4);
    u32x4 B[4];
#pragma unroll
    for (int i = 0; i < 4; ++i) B[i] = ld128(ao + (z - 14) * 4096 + i * 1024);
    stage(z, B);
  }
  if (BID == 32 && TID == 0) __hip_atomic_store((GAS u32*)(ws + WS_EP), tag, RLX_AGENT);
  f32x16 mine = {};
#pragma unroll
  for (int t = 0; t < 4; ++t) {
    const f32x16& a = acc[t & 1][t >> 1];
    if (t == w) mine = a;
    else {
#pragma unroll
      for (int g = 0; g < 4; ++g)
        *(LAS f32x4*)(lb + thome(t + 1) + w * 4096 + g * 1024 + l * 16) = f32x4{a[4 * g], a[4 * g + 1], a[4 * g + 2], a[4 * g + 3]};
    }
  }
  BAR();
  const u32 rb = lb + (w < 2 ? thome(1) : thome(3)) + (w & 1) * 16384 + l * 16;
#pragma unroll
  for (int o = 1; o < 4; ++o) {
    const u32 src = rb + ((w + o) & 3) * 4096;
#pragma unroll
    for (int g = 0; g < 4; ++g) {
      const f32x4 v = *(LAS f32x4*)(src + g * 1024);
#pragma unroll
      for (int e = 0; e < 4; ++e) mine[4 * g + e] += v[e];
    }
  }
#pragma unroll
  for (int g = 0; g < 4; ++g)
    *(u32x2*)(out + orow + 8 * g) = u32x2{pk2(mine[4 * g] + lo16(xr[g][0]), mine[4 * g + 1] + hi16(xr[g][0])),
                                          pk2(mine[4 * g + 2] + lo16(xr[g][1]), mine[4 * g + 3] + hi16(xr[g][1]))};
  STAMP(10);
}

#if defined(__HIP_DEVICE_COMPILE__)
#pragma clang attribute pop
#endif

extern "C" void kernel_launch(void* const* d_in, const int* in_sizes, int n_in, void* d_out, int out_size, void* d_ws, size_t ws_size,
                              hipStream_t stream) {
  fused<<<256, 512, 0, stream>>>((const u16*)d_in[0], (const u16*)d_in[1], (const u16*)d_in[2], (const u16*)d_in[3], (const u16*)d_in[5],
                                 (const u16*)d_in[6], (const u16*)d_in[7], (char*)d_ws, (u16*)d_out);
}
```

```cpp
#define PROBE 1
#include <hip/hip_runtime.h>
#pragma clang diagnostic ignored "-Wint-to-pointer-cast"

#if defined(__HIP_DEVICE_COMPILE__)
#pragma clang attribute push(__attribute__((target("no-packed-fp32-ops"))), apply_to = function)
#endif
#define TID ((int)__builtin_amdgcn_workitem_id_x())
#define BID ((int)__builtin_amdgcn_workgroup_id_x())
#define ALL(c) (__builtin_amdgcn_ballot_w64(!(c)) == 0)
#define F2U(x) __builtin_bit_cast(u32, (float)(x))
#define U2F(x) __builtin_bit_cast(float, (u32)(x))

typedef unsigned short u16;
typedef unsigned int u32;
typedef unsigned long long u64;
typedef __attribute__((ext_vector_type(8))) __bf16 bf16x8;
typedef __attribute__((ext_vector_type(2))) __bf16 bf16x2;
typedef __attribute__((ext_vector_type(4))) short s16x4;
typedef __attribute__((ext_vector_type(16))) float f32x16;
typedef __attribute__((ext_vector_type(4))) float f32x4;
typedef __attribute__((ext_vector_type(4))) u32 u32x4;
typedef __attribute__((ext_vector_type(2))) u32 u32x2;
#define LAS __attribute__((address_space(3)))
#define GAS __attribute__((address_space(1)))
#define DI __device__ __forceinline__
#define SBAR() __builtin_amdgcn_sched_barrier(0)
#define PIN(x) asm volatile("" : "+v"(x))
#define RLX_AGENT __ATOMIC_RELAXED, __HIP_MEMORY_SCOPE_AGENT

constexpr int S_ = 2048, D_ = 448, F_ = 1792;
constexpr u32 WS_H = 0;
constexpr u32 WS_PQ = WS_H + 64 * 112 * 1024;
constexpr u32 WS_KVP = WS_PQ + 64 * 16 * 2048;
constexpr u32 WS_KVF = WS_KVP + 32 * 16 * 4096;
constexpr u32 WS_CS = WS_KVF + 65536;
constexpr u32 WS_ST = WS_CS + 16 * 10 * 2048;
constexpr u32 WS_FH1 = WS_ST + 2048 * 8;
constexpr u32 WS_FH2 = WS_FH1 + 256 * 128;
constexpr u32 WS_FKV = WS_FH2 + 256 * 128;
constexpr u32 WS_FR = WS_FKV + 512 * 128;
constexpr u32 WS_EP = WS_FR + 33 * 128;
constexpr u32 WS_END = WS_EP + 512;
constexpr u32 WS_DBG = WS_END;

#define STAMP(k) do { if ((k) == 0 || (k) == PROBE) { u64 t_; asm volatile("s_memrealtime %0\n\ts_waitcnt lgkmcnt(0)" : "=s"(t_) :: "memory"); if ((k) == 0) pt0 = t_; else pt1 = t_; } \
  if ((k) == 10 && BID == 40 && wid == 0) { u64 e_ = __builtin_amdgcn_s_memrealtime() + 4 * (pt1 - pt0); while (__builtin_amdgcn_s_memrealtime() < e_) __builtin_amdgcn_s_sleep(1); } } while (0)
#define STAMPC(k) do {} while (0)

DI f32x16 mfma(u32x4 a, u32x4 b, f32x16 c) {
  return __builtin_amdgcn_mfma_f32_32x32x16_bf16(__builtin_bit_cast(bf16x8, a), __builtin_bit_cast(bf16x8, b), c, 0, 0, 0);
}
DI u32 pk2(float lo, float hi) { bf16x2 p; p[0] = (__bf16)lo; p[1] = (__bf16)hi; return __builtin_bit_cast(u32, p); }
DI float rbf(float f) { return (float)(__bf16)f; }
DI float lo16(u32 u) { return U2F(u << 16); }
DI float hi16(u32 u) { return U2F(u & 0xffff0000u); }
DI float dot2(u32 a, u32 b, float c) {
  return __builtin_amdgcn_fdot2_f32_bf16(__builtin_bit_cast(bf16x2, a), __builtin_bit_cast(bf16x2, b), c, false);
}
DI u32x2 ldtr(u32 addr) { return __builtin_bit_cast(u32x2, __builtin_amdgcn_ds_read_tr16_b64_v4i16((LAS s16x4*)addr)); }
DI u32x4 ldtr2(u32 a0, u32 a1) { u32x2 a = ldtr(a0), b = ldtr(a1); return u32x4{a[0], a[1], b[0], b[1]}; }
DI u32x4 ld128(u32 addr) { return *(LAS u32x4*)addr; }
DI void st128(u32 addr, u32x4 v) { *(LAS u32x4*)addr = v; }
DI void st64(u32 addr, u32x2 v) { *(LAS u32x2*)addr = v; }
DI u32x4 mksrd(const void* p, u32 bytes) {
  u64 a = (u64)p;
  return u32x4{(u32)a, (u32)(a >> 32) & 0xffffu, bytes, 0x00020000u};
}
DI void dma1(u32x4 srd, u32 v0, u32 soff, u32 lds) {
  u32 k;
  asm volatile("s_mov_b32 %0, m0\n\ts_mov_b32 m0, %4\n\ts_nop 0\n\tbuffer_load_dwordx4 %1, %2, %3 offen lds\n\ts_mov_b32 m0, %0"
               : "=&s"(k) : "v"(v0), "s"(srd), "s"(soff), "s"(lds) : "memory");
}
DI void dma2(u32x4 srd, u32 v0, u32 v1, u32 soff, u32 lds) {
  u32 k;
  asm volatile("s_mov_b32 %0, m0\n\ts_mov_b32 m0, %5\n\ts_nop 0\n\tbuffer_load_dwordx4 %1, %3, %4 offen lds\n\tbuffer_load_dwordx4 %2, %3, %4 offen offset:1024 lds\n\t"
               "s_mov_b32 m0, %0" : "=&s"(k) : "v"(v0), "v"(v1), "s"(srd), "s"(soff), "s"(lds) : "memory");
}
DI void dma4(u32x4 srd, u32 v0, u32 v1, u32 v2, u32 v3, u32 soff, u32 lds) {
  u32 k;
  asm volatile("s_mov_b32 %0, m0\n\ts_mov_b32 m0, %7\n\ts_nop 0\n\tbuffer_load_dwordx4 %1, %5, %6 offen lds\n\tbuffer_load_dwordx4 %2, %5, %6 offen offset:1024 lds\n\t"
               "buffer_load_dwordx4 %3, %5, %6 offen offset:2048 lds\n\tbuffer_load_dwordx4 %4, %5, %6 offen offset:3072 lds\n\ts_mov_b32 m0, %0"
               : "=&s"(k) : "v"(v0), "v"(v1), "v"(v2), "v"(v3), "s"(srd), "s"(soff), "s"(lds) : "memory");
}
#define WAITV(n) asm volatile("s_waitcnt vmcnt(%0)" ::"i"(n) : "memory")
#define BAR() do { asm volatile("" ::: "memory"); __builtin_amdgcn_s_waitcnt(0xC07F); __builtin_amdgcn_s_barrier(); asm volatile("" ::: "memory"); SBAR(); } while (0)
DI void xhalf(float v, float& lo, float& hi) {
  auto r = __builtin_amdgcn_permlane32_swap(F2U(v), F2U(v), false, false);
  lo = U2F(r[0]); hi = U2F(r[1]);
}
DI float xhalf_hi(float v) { float lo, hi; xhalf(v, lo, hi); return hi; }
constexpr int SPIN_CAP = 1 << 20;
DI void wait_flags(const char* p, u32 tag) {
#pragma unroll 1
  for (int spin = 0; spin < SPIN_CAP; ++spin) {
    const u32 v = __hip_atomic_load((GAS u32*)p, RLX_AGENT);
    if (ALL(v == tag)) break;
    __builtin_amdgcn_s_sleep(2);
  }
  __builtin_amdgcn_fence(__ATOMIC_ACQUIRE, "wavefront");
}
DI u32 lds_inc(u32 addr) {
  u32 o = 0;
  if ((TID & 63) == 0) o = __hip_atomic_fetch_add((LAS u32*)addr, 1u, __ATOMIC_RELAXED, __HIP_MEMORY_SCOPE_WORKGROUP);
  return __builtin_amdgcn_readfirstlane(o);
}
template <typename R> DI void st_wt(R rs, u32 off, u32x4 v) { __builtin_amdgcn_raw_buffer_store_b128(v, rs, off, 0, 16); }
DI void lds_wait(u32 addr, u32 n) {
#pragma unroll 1
  for (int spin = 0; spin < SPIN_CAP; ++spin)
    if (__builtin_amdgcn_readfirstlane(__hip_atomic_load((LAS u32*)addr, __ATOMIC_RELAXED, __HIP_MEMORY_SCOPE_WORKGROUP)) >= n) break;
  asm volatile("" ::: "memory");
}
DI void lds_sig(u32 addr) {
  asm volatile("" ::: "memory");
  *(volatile LAS u32*)addr = 1u;
  asm volatile("" ::: "memory");
}
DI void lds_wait4(u32 addr) {
#pragma unroll 1
  for (int spin = 0; spin < SPIN_CAP; ++spin) {
    const u32x4 v = *(volatile LAS u32x4*)addr;
    if (__builtin_amdgcn_readfirstlane(v[0] & v[1] & v[2] & v[3])) break;
  }
  asm volatile("" ::: "memory");
}
typedef __attribute__((ext_vector_type(2))) float f32x2;
DI float max3(float a, float b, float c) { float d; asm("v_max3_f32 %0, %1, %2, %3" : "=v"(d) : "v"(a), "v"(b), "v"(c)); return d; }
DI float min3(float a, float b, float c) { float d; asm("v_min3_f32 %0, %1, %2, %3" : "=v"(d) : "v"(a), "v"(b), "v"(c)); return d; }
DI float ex2(float a) { return __builtin_amdgcn_exp2f(a); }
#define TK(v)
constexpr float LOG2E = 1.4426950408889634f;

constexpr u32 L_SLOT = 40960, L_A = 8192, L_X = 24576;
constexpr u32 L_VF = 0, L_PHI = L_SLOT;
constexpr u32 L_R4 = 122880, L_WPQ = 139264, L_WPK = L_WPQ + 7168, L_FIX = L_WPK + 2048, L_ST = L_FIX + 10240, L_CTL = L_ST + 1024;
constexpr u32 C_A = L_CTL + 4, C_B = L_CTL + 8, C_R = L_CTL + 12, C_KV = L_CTL + 16, C_T1 = L_CTL + 32, C_T2 = L_CTL + 48, C_P1 = L_CTL + 64, C_P2 = L_CTL + 80;
DI constexpr u32 thome(int i) { return i == 0 ? L_R4 : (i < 3 ? L_SLOT + L_A + (i - 1) * 16384 : (i < 5 ? 2 * L_SLOT + L_A + (i - 3) * 16384 : L_A + (i - 5) * 16384)); }
constexpr int CODE_LINES = 256;

__global__ __launch_bounds__(512, 2) void fused(const u16* __restrict__ x, const u16* __restrict__ wqkv, const u16* __restrict__ wphi,
                                                const u16* __restrict__ wo, const u16* __restrict__ wg, const u16* __restrict__ wu,
                                                const u16* __restrict__ wd, char* __restrict__ ws, u16* __restrict__ out) {
  __shared__ __attribute__((aligned(16))) char smem[163840];
  u64 pt0 = 0, pt1 = 0;
  u64 pc0; asm volatile("s_getpc_b64 %0" : "=s"(pc0));
  const u32 lb = (u32)(size_t)(LAS char*)smem;
  const int wid = __builtin_amdgcn_readfirstlane(TID >> 6);
  const int l = TID & 63;
  const int xcd = BID & 7, idx = BID >> 3;
  const int panel = (xcd >> 1) * 4 + (idx & 3);
  const int j = (xcd & 1) * 8 + (idx >> 2);
  const bool ytype = (j < 2) || (j >= 14);
  const int qg = j < 2 ? j : j - 12;
  const int line1 = 2 * j - 2;
  const int line2 = j < 2 ? j : (j >= 14 ? j + 12 : 2 * j - 1);
  const int koff = (j & 1) * 8;
  const int rot = (2 * panel + j) % 7;
  const int blk = (l >> 4) & 1, q = (l & 15) >> 2, p = l & 3, h = l >> 5, r = l & 31;
  const u32 rowpart = (8 * h + q) * 128, rx = 64 * ((q >> 1) & 1);
  const auto rsw = __builtin_amdgcn_make_buffer_rsrc((void*)ws, 0, WS_END, 0x00020000);
  auto ffn_addr = [&](int uu) { return lb + blk * 8192 + rowpart + ((32 * uu + 8 * p) ^ (32 * blk) ^ rx); };
  auto q_addr = [&](int c) { const u32 o = ((qg == 3 ? 32 : 0) + 56 * c + 32 * blk + 8 * p) & 255; return lb + (o >> 7) * 8192 + rowpart + ((o & 127) ^ rx); };
  const u32 k_addr = lb + rowpart + (((koff + 32 * blk + 8 * p) & 63) ^ rx);
  const u32 lc = (16 * (l & 7)) ^ (64 * ((l >> 4) & 1));
  auto xfix = [&](int t) { const u32 nm = *(LAS u32*)(lb + L_ST + 512 + (32 * t + r) * 4); return u32x4{nm, nm, nm, h ? 0u : nm}; };
  auto ffn = [&](const f32x16& a, float rs, int tile, int unit) {
    const float r2 = rs * rs;
    u32x4 o;
#pragma unroll
    for (int e = 0; e < 4; ++e) o[e] = pk2(__builtin_fmaxf(a[2 * e], 0.f) * a[8 + 2 * e] * r2, __builtin_fmaxf(a[2 * e + 1], 0.f) * a[9 + 2 * e] * r2);
    st_wt(rsw, l * 16 + WS_H + ((panel * 4 + tile) * 112 + unit) * 1024, o);
  };
#define MM(c, a, b) do { c = mfma(a, b, c); PIN(c); SBAR(); } while (0)
#define RW(Wr, a, no) do { Wr = ldtr2((a) + (no), (a) + (no) + 512); } while (0)
  const bool reducer = idx < 4;
  const int ti = xcd * 4 + ((idx - 4) & 3), ct = (idx - 4) >> 2;
  STAMP(0);

  if (wid >= 4) {
    const int w = wid - 4;
    const u32x4 srdQ = mksrd(wqkv, D_ * 3 * D_ * 2), srdG = mksrd(wg, D_ * F_ * 2), srdU = mksrd(wu, D_ * F_ * 2), srdX = mksrd(x, S_ * D_ * 2);
    const u32 vkv = (l >> 3) * 2688 + 896 + 56 * j - koff + (lc < 64 ? lc : lc + 896 - 64);
    const u32 vx0 = (l >> 3) * 896 + 16 * ((l & 7) ^ (l >> 4)), vx1 = (l >> 3) * 896 + 16 * ((l & 7) ^ (4 + (l >> 4))) + 6144;
    const u32 xrow0 = (panel * 128 + w * 32) * 896;
    const u32 st1 = ytype ? 2688 : 3584;
    const u32x4 srdA1 = ytype ? srdQ : srdG, srdB1 = ytype ? srdQ : srdU;
    const u32 va1 = (l >> 3) * st1 + (ytype ? (qg == 3 ? 640 : 224 * qg) : line1 * 128) + lc;
    const u32 vb1 = ytype ? va1 + 128 : (va1 ^ 32);
    const u32 va2 = (l >> 3) * 3584 + line2 * 128 + lc, vb2 = va2 ^ 32;
    auto kblk = [&](int i) { const int kb = rot + i; return kb >= 7 ? kb - 7 : kb; };
    auto issueS = [&](int i) {
      const int kb = kblk(i); const u32 slot = lb + (i % 3) * L_SLOT;
      dma4(srdX, vx0, vx1, vx0 + 2 * 6144, vx1 + 2 * 6144, xrow0 + kb * 128, slot + L_X + w * 4096);
      dma2(srdA1, va1, va1 + 8 * st1 - 1024, (64 * kb + 16 * w) * st1, slot + L_A + w * 2048);
      dma2(srdB1, vb1, vb1 + 8 * st1 - 1024, (64 * kb + 16 * w) * st1, slot + L_A + 8192 + w * 2048);
      dma2(srdQ, vkv, vkv + 8 * 2688 - 1024, (64 * kb + 16 * w) * 2688, slot + w * 2048);
    };
    auto issueT = [&](int i) {
      const int kb = kblk(i);
      dma2(srdG, va2, va2 + 8 * 3584 - 1024, (64 * kb + 16 * w) * 3584, lb + thome(i) + w * 2048);
      dma2(srdU, vb2, vb2 + 8 * 3584 - 1024, (64 * kb + 16 * w) * 3584, lb + thome(i) + 8192 + w * 2048);
    };
    issueS(0); issueS(1);
    const bool prod = panel < 7;
    const u32 csb = WS_CS + j * 20480;
    u32 tag;
    auto rd4 = [&](u32x4 (&f)[4], u32 a) {
#pragma unroll
      for (int k = 0; k < 4; ++k) f[k] = ldtr2(a + k * 2048, a + k * 2048 + 512);
    };
    auto pubcs = [&](const u32x4 (&f)[4], int unit) {
      const u32x4 ones = {0x3f803f80u, 0x3f803f80u, 0x3f803f80u, 0x3f803f80u};
      f32x16 c = {};
#pragma unroll
      for (int k = 0; k < 4; ++k) c = mfma(ones, f[k], c);
      const float s = c[0], s0 = rbf(s);
      if (l < 32) __builtin_amdgcn_raw_buffer_store_b64(u32x2{pk2(s0, s - s0), tag}, rsw, r * 8, csb + unit * 2048 + rot * 256, 16);
    };
    const u32 goff = h * 1024 + r * 8;
    const u32x4 srdS = mksrd(ws, WS_END);
    auto gather = [&](u32x2 (&g)[4], int unit) {
      asm volatile("buffer_load_dwordx2 %0, %4, %6, %7 offen sc1\n\tbuffer_load_dwordx2 %1, %4, %6, %7 offen offset:256 sc1\n\t"
                   "buffer_load_dwordx2 %2, %4, %6, %7 offen offset:512 sc1\n\tbuffer_load_dwordx2 %3, %5, %6, %7 offen offset:768 sc1"
                   : "=&v"(g[0]), "=&v"(g[1]), "=&v"(g[2]), "=&v"(g[3]) : "v"(goff), "v"(goff - h * 256), "s"(srdS), "s"(csb + unit * 2048) : "memory");
    };
    u32x4 X[7][4];
    const u32 ax = lb + L_X + (32 * w + r) * 128 + 16 * (h ^ ((r >> 1) & 7));
    auto keepx = [&](int i) {
#pragma unroll
      for (int k = 0; k < 4; ++k) X[i][k] = ld128((ax ^ (32 * k)) + (i % 3) * L_SLOT);
    };
    u32x4 f0[4], f1[4];
    TK(e0); WAITV(10); BAR();
    TK(k0);
    keepx(0);
    if (prod) { rd4(f0, (ytype ? q_addr(w) : ffn_addr(w)) + L_A); rd4(f1, k_addr ^ (64 * (w & 1))); }
    issueT(0); issueS(2);
    tag = __builtin_amdgcn_readfirstlane(*(LAS u32*)(lb + L_CTL));
    if (prod) pubcs(f0, w);
    TK(e1); WAITV(10); BAR();
    TK(k1);
    keepx(1);
    if (prod) rd4(f0, ffn_addr(w) + thome(0));
    issueS(3);
    if (prod) { if (w < 2) pubcs(f1, 4 + w); pubcs(f0, 6 + w); }
    TK(e2); WAITV(10); BAR();
    TK(k2);
    keepx(2); issueS(4);
    TK(e3); WAITV(10); BAR();
    TK(k3);
    keepx(3); issueS(5);
    TK(e4); WAITV(10); BAR();
    TK(k4);
    keepx(4); issueS(6);
    u32x2 g0[4], g1[4], g2[4], s2;
    auto gather_all = [&]() {
      gather(g0, w); gather(g1, 4 + (w & 1)); gather(g2, 6 + w);
      asm volatile("buffer_load_dwordx2 %0, %1, %2, %3 offen sc1" : "=&v"(s2) : "v"(r * 8), "s"(srdS), "s"(WS_ST + (panel * 128 + w * 32) * 8) : "memory");
    };
#define WAITG(n) asm volatile("s_waitcnt vmcnt(%13)" : "+v"(g0[0]), "+v"(g0[1]), "+v"(g0[2]), "+v"(g0[3]), "+v"(g1[0]), "+v"(g1[1]), "+v"(g1[2]), "+v"(g1[3]), \
                              "+v"(g2[0]), "+v"(g2[1]), "+v"(g2[2]), "+v"(g2[3]), "+v"(s2) : "i"(n) : "memory")
    auto gbad = [&]() {
      u32 bad = s2[1] ^ tag;
#pragma unroll
      for (int i = 0; i < 4; ++i) bad |= (g0[i][1] ^ tag) | (g1[i][1] ^ tag) | (g2[i][1] ^ tag);
      return !ALL(bad == 0u);
    };
    WAITV(10); BAR();
    TK(k5);
    keepx(5); gather_all(); issueT(1);
    TK(e6); WAITV(13 + 4); BAR();
    TK(k6);
    keepx(6); issueT(2); issueT(3);
    WAITG(12);
#pragma unroll 1
    for (int spin = 0; spin < SPIN_CAP && gbad(); ++spin) { __builtin_amdgcn_s_sleep(1); gather_all(); WAITG(0); }
    st128(lb + L_FIX + w * 1024 + l * 16, u32x4{g0[0][0], g0[1][0], g0[2][0], g0[3][0]});
    st128(lb + L_FIX + 4096 + (w & 1) * 1024 + l * 16, u32x4{g1[0][0], g1[1][0], g1[2][0], g1[3][0]});
    st128(lb + L_FIX + 6144 + w * 1024 + l * 16, u32x4{g2[0][0], g2[1][0], g2[2][0], g2[3][0]});
    *(LAS u32*)(lb + L_ST + h * 512 + (32 * w + r) * 4) = h ? ((s2[0] & 0xffffu) ^ 0x8000u) * 0x10001u : F2U(__builtin_amdgcn_rcpf(hi16(s2[0])));
    TK(e7); WAITV(8); BAR();
    TK(k7);
    issueT(4); issueT(5); issueT(6);
#define BPRIO 2
    __builtin_amdgcn_s_setprio(BPRIO);
    STAMP(2); STAMPC(14);
    f32x16 c0 = {}, c1 = {}, c2 = {}, c3 = {};
    u32x4 W0, W1, W2, W3, W0n, W1n, W2n, W3n;
    u32 b0 = ffn_addr(0) + thome(0), b1 = ffn_addr(1) + thome(0), b2 = ffn_addr(2) + thome(0), b3 = ffn_addr(3) + thome(0);
    PIN(b0); PIN(b1); PIN(b2); PIN(b3);
    RW(W0, b0, 0u); RW(W1, b1, 0u); RW(W2, b2, 0u); RW(W3, b3, 0u);
    u32 fl = 0;
    bool early = false;
    u32x4 pq[4][2][2];
    auto loadpq = [&]() {
      __builtin_amdgcn_fence(__ATOMIC_ACQUIRE, "wavefront");
#pragma unroll
      for (int c = 0; c < 4; ++c)
#pragma unroll
        for (int b = 0; b < 2; ++b)
#pragma unroll
          for (int s = 0; s < 2; ++s)
            pq[c][b][s] = __builtin_amdgcn_raw_buffer_load_b128(rsw, l * 16, WS_PQ + ((2 * ti + b) * 16 + 4 * w + c) * 2048 + s * 1024, 16);
    };
    const u32 fx = lb + L_FIX + 6144 + l * 16;
#pragma unroll
    for (int i = 0; i < 7; ++i) {
#pragma unroll
      for (int k = 0; k < 4; ++k) {
        if (k == 0 && i == 1) { WAITV(12); lds_sig(lb + C_T1 + 4 * w); }
        if (k == 0 && i == 3) { WAITV(0); lds_sig(lb + C_T2 + 4 * w); }
        if (k == 1 && i == 3) lds_sig(lb + C_P1 + 4 * w);
        if (k == 1 && i == 5) lds_sig(lb + C_P2 + 4 * w);
        if (k == 0 && i == 5 && !reducer) fl = __hip_atomic_load((GAS u32*)(ws + WS_FH1 + ((ti >> 1) * 16 + (w < 2 ? w : w + 12)) * 128), RLX_AGENT);
        if (k == 1 && i == 6 && !reducer && __builtin_amdgcn_readfirstlane(fl) == tag) { early = true; loadpq(); }
        if (k == 3 && i < 6) {
          if (i == 1) lds_wait4(lb + C_T1);
          if (i == 3) lds_wait4(lb + C_T2);
          const u32 d = thome(i + 1) - thome(i);
          b0 += d; b1 += d; b2 += d; b3 += d; PIN(b0); PIN(b1); PIN(b2); PIN(b3);
        }
        const u32 no = k == 3 ? 0u : (u32)(k + 1) * 2048u;
        const bool last = i == 6 && k == 3;
        SBAR();
        if (last) W0n = ld128(fx); else RW(W0n, b0, no);
        SBAR(); MM(c0, W0, X[i][k]);
        if (last) W1n = ld128(fx + 1024); else RW(W1n, b1, no);
        SBAR(); MM(c1, W1, X[i][k]);
        if (last) W2n = ld128(fx + 2048); else RW(W2n, b2, no);
        SBAR(); MM(c2, W2, X[i][k]);
        if (last) W3n = ld128(fx + 3072); else RW(W3n, b3, no);
        SBAR(); MM(c3, W3, X[i][k]);
        W0 = W0n; W1 = W1n; W2 = W2n; W3 = W3n;
      }
    }
    const u32x4 M0 = xfix(w);
    const float rs0 = *(LAS float*)(lb + L_ST + (32 * w + r) * 4);
    MM(c0, W0, M0); MM(c1, W1, M0); MM(c2, W2, M0); MM(c3, W3, M0);
    STAMP(3); STAMPC(15);
    ffn(c0, rs0, w, line2 * 4); ffn(c1, rs0, w, line2 * 4 + 1); ffn(c2, rs0, w, line2 * 4 + 2); ffn(c3, rs0, w, line2 * 4 + 3);
    WAITV(0);
    if (lds_inc(lb + C_B) == 3u && l == 0) __hip_atomic_store((GAS u32*)(ws + WS_FH2 + (panel * 16 + j) * 128), tag, RLX_AGENT);
    STAMP(4);
    if (reducer) return;
    if (!early) { wait_flags(ws + WS_FH1 + ((ti >> 1) * 16 + (w < 2 ? w : w + 12)) * 128, tag); loadpq(); }
    u32x4 ef[4][2][4];
    auto soft = [&](int c, int b) {
      float mx, mn, t_;
      xhalf(U2F(pq[c][b][1][2]), t_, mx);
      mn = hi16(F2U(mx)) * LOG2E; mx = lo16(F2U(mx)) * -LOG2E;
#pragma unroll
      for (int e = 0; e < 8; ++e) {
        const u32 u = pq[c][b][e >> 2][e & 3];
        const f32x2 v = {lo16(u), hi16(u)};
        const f32x2 ap = v * LOG2E + mx, an = v * -LOG2E + mn;
        ef[c][b][e >> 2][e & 3] = pk2(ex2(ap[0]), ex2(ap[1])); ef[c][b][2 + (e >> 2)][e & 3] = pk2(ex2(an[0]), ex2(an[1]));
      }
#pragma unroll
      for (int s = 0; s < 4; ++s) PIN(ef[c][b][s]);
      SBAR();
    };
    soft(0, 0); soft(0, 1);
    STAMP(5);
    soft(1, 0); soft(1, 1); soft(2, 0); soft(2, 1);
    STAMP(6);
    wait_flags(ws + WS_FR + (8 * w + (l & 7)) * 128, tag);
    STAMP(7);
    u32x4 kv[4][4];
#pragma unroll
    for (int c = 0; c < 4; ++c)
#pragma unroll
      for (int s = 0; s < 4; ++s) kv[c][s] = __builtin_amdgcn_raw_buffer_load_b128(rsw, l * 16, WS_KVF + (4 * w + c) * 4096 + s * 1024, 16);
    SBAR();
    soft(3, 0); soft(3, 1);
    lds_wait(lb + C_B, 4u); lds_wait4(lb + C_KV);
    STAMP(8);
    const u32 ob = lb + (w < 3 ? w * 16384 : L_WPQ) + l * 16;
#pragma unroll
    for (int c = 0; c < 4; ++c)
#pragma unroll
      for (int b = 0; b < 2; ++b) {
        f32x16 ap = {}, an = {};
        ap = mfma(kv[c][0], ef[c][b][0], ap); an = mfma(kv[c][2], ef[c][b][2], an);
        ap = mfma(kv[c][1], ef[c][b][1], ap); an = mfma(kv[c][3], ef[c][b][3], an);
        const float rp = __builtin_amdgcn_rcpf(ap[13]), rn = __builtin_amdgcn_rcpf(an[13]);
        const float rd = __builtin_amdgcn_rcpf(ap[12] * rp + an[12] * rn + 1e-6f);
        const float ca = xhalf_hi(rp * rd), cb = xhalf_hi(rn * rd);
        u32x4 o0, o1;
#pragma unroll
        for (int e = 0; e < 4; ++e) {
          o0[e] = pk2(ap[2 * e] * ca + an[2 * e] * cb, ap[2 * e + 1] * ca + an[2 * e + 1] * cb);
          o1[e] = pk2(ap[8 + 2 * e] * ca + an[8 + 2 * e] * cb, ap[9 + 2 * e] * ca + an[9 + 2 * e] * cb);
        }
        o1[2] = h ? 0u : o1[2]; o1[3] = h ? 0u : o1[3];
        st128(ob + c * 4096 + b * 2048, o0); st128(ob + c * 4096 + b * 2048 + 1024, o1);
      }
    STAMP(9);
    BAR();
    return;
  }

  const int m = wid >> 1, n = wid & 1;
  const int t0 = 2 * m + n, t1 = 2 * m + 1 - n;
  if (wid == 0) {
    const u32 ep = __hip_atomic_load((GAS u32*)(ws + WS_EP), RLX_AGENT);
    const u32x4* src = (const u32x4*)(x + (panel * 128 + j * 8 + (l >> 3)) * D_) + (l & 7);
    u32x4 xs[7];
#pragma unroll
    for (int i = 0; i < 7; ++i) xs[i] = src[i * 8];
    const u32 tag0 = __builtin_amdgcn_readfirstlane(ep) * 1664525u + 1013904223u;
    if (l < 32) *(LAS u32*)(lb + L_CTL + 4 * l) = l == 0 ? tag0 : 0u;
    float sx = 0.f, sq = 0.f;
#pragma unroll
    for (int i = 0; i < 7; ++i)
#pragma unroll
      for (int e = 0; e < 4; ++e) { sx = dot2(xs[i][e], 0x3f803f80u, sx); sq = dot2(xs[i][e], xs[i][e], sq); }
#pragma unroll
    for (int o = 1; o < 8; o <<= 1) { sx += U2F(__builtin_amdgcn_ds_bpermute((l ^ o) * 4, F2U(sx))); sq += U2F(__builtin_amdgcn_ds_bpermute((l ^ o) * 4, F2U(sq))); }
    const float mean = sx * (1.f / D_);
    const float var = __builtin_fmaxf(sq * (1.f / D_) - mean * mean, 0.f);
    const float sd = __builtin_amdgcn_sqrtf(rbf(rbf(var) + 1e-5f));
    if ((l & 7) == 0) __hip_atomic_store((GAS u64*)(ws + WS_ST + (panel * 128 + j * 8 + (l >> 3)) * 8), (u64)pk2(mean, sd) | ((u64)tag0 << 32), RLX_AGENT);
  } else if (wid == 1) {
    const u32x4 srdP = mksrd(wphi, 16 * 784 * 2);
    dma2(srdP, l * 16, l * 16, j * 1568, lb + L_WPK);
    if (ytype) { dma4(srdP, l * 16, l * 16, l * 16, l * 16, qg * 6272, lb + L_WPQ); dma2(srdP, l * 16, l * 16, qg * 6272 + 4096, lb + L_WPQ + 4096); dma1(srdP, l * 16, qg * 6272 + 6144, lb + L_WPQ + 6144); }
    WAITV(0);
  }
  else if (wid == 2) {
    const int cl = idx * 8 + (l & 7);
    u32 t = 0;
    if (l < 8 && cl < CODE_LINES) t = *(const volatile u32*)((pc0 & ~127ull) + cl * 128);
    const int wl = BID * 64 + l;
    if (wl < 12544) t += *(const volatile u32*)((const char*)wd + wl * 128);
    else if (wl < 15680) t += *(const volatile u32*)((const char*)wo + (wl - 12544) * 128);
    asm volatile("" ::"v"(t));
  }
  u32 a0 = (ytype ? q_addr(2 * n) : ffn_addr(2 * n)) + L_A, a1 = (ytype ? q_addr(2 * n + 1) : ffn_addr(2 * n + 1)) + L_A;
  u32 ak = k_addr, av = k_addr ^ 64;
  u32 ax0 = lb + L_X + (32 * t0 + r) * 128 + 16 * (h ^ ((r >> 1) & 7));
  u32 ax1 = ax0 ^ 4096;
  f32x16 c00 = {}, c01 = {}, c10 = {}, c11 = {}, ck = {}, cv = {};
  u32x4 X0 = {}, X1 = {}, W0 = {}, W1 = {}, WK = {}, WV = {}, X0n, X1n, W0n, W1n, WKn, WVn;
#define XSTEP(xx) do { ax0 ^= (xx); ax1 ^= (xx); PIN(ax0); PIN(ax1); } while (0)
#define REST1(no) do { RW(W0n, a0, no); X0n = ld128(ax0); SBAR(); MM(c01, W0, X1); X1n = ld128(ax1); RW(W1n, a1, no); SBAR(); MM(c10, W1, X0); \
    RW(WKn, ak, no); SBAR(); MM(c11, W1, X1); RW(WVn, av, no); SBAR(); MM(ck, WK, X0); MM(cv, X0, WV); \
    X0 = X0n; X1 = X1n; W0 = W0n; W1 = W1n; WK = WKn; WV = WVn; } while (0)
  PIN(c00); PIN(c01); PIN(c10); PIN(c11); PIN(ck); PIN(cv); PIN(X0); PIN(X1); PIN(W0); PIN(W1); PIN(WK); PIN(WV);
  BAR();
  STAMP(1); STAMPC(14);
  int ph = 0;
#pragma unroll 1
  for (int it = 0; it < 7; ++it) {
    REST1(0u);
    XSTEP(32u); MM(c00, W0, X0); REST1(2048u);
    XSTEP(96u); MM(c00, W0, X0); REST1(4096u);
    XSTEP(32u); MM(c00, W0, X0); REST1(6144u);
    const u32 delta = ph == 2 ? 0u - 2 * L_SLOT : L_SLOT; ph = ph == 2 ? 0 : ph + 1;
    ax0 = (ax0 ^ 96u) + delta; ax1 = (ax1 ^ 96u) + delta; a0 += delta; a1 += delta; ak += delta; av += delta;
    PIN(ax0); PIN(ax1); PIN(a0); PIN(a1); PIN(ak); PIN(av);
    SBAR();
    MM(c00, W0, X0); BAR();
  }
  const u32 fx = lb + L_FIX + l * 16;
  WKn = ld128(fx + 4096); WVn = ld128(fx + 5120); W0n = ld128(fx + n * 2048); W1n = ld128(fx + n * 2048 + 1024);
  const u32x4 M0 = xfix(t0), M1 = xfix(t1);
  const float rs0 = *(LAS float*)(lb + L_ST + (32 * t0 + r) * 4), rs1 = *(LAS float*)(lb + L_ST + (32 * t1 + r) * 4);
  const u32 tag = __builtin_amdgcn_readfirstlane(*(LAS u32*)(lb + L_CTL));
  SBAR();
  MM(c01, W0, X1); MM(c10, W1, X0); MM(c11, W1, X1); MM(ck, WK, X0); MM(cv, X0, WV);
  STAMP(2); STAMPC(15);
  MM(c00, W0n, M0); MM(c01, W0n, M1); MM(c10, W1n, M0); MM(c11, W1n, M1); MM(ck, WKn, M0); MM(cv, M0, WVn);
  const u32 wpl = (4 * h + q) * 56 + 32 * blk + 8 * p;
  auto prephi = [&](const f32x16& a, float rs, u32 wp, float& mx, float& mn) {
    u32x4 b0, b1;
#pragma unroll
    for (int e = 0; e < 4; ++e) { b0[e] = pk2(a[2 * e] * rs, a[2 * e + 1] * rs); b1[e] = pk2(a[8 + 2 * e] * rs, a[9 + 2 * e] * rs); }
    b1[2] = h ? 0u : b1[2]; b1[3] = h ? 0u : b1[3];
    f32x16 pz = {};
    pz = mfma(ldtr2(wp, wp + 8 * 56), b0, pz);
    pz = mfma(ldtr2(wp + 16 * 56, wp + 24 * 56), b1, pz);
    float x1 = __builtin_fmaxf(pz[0], pz[1]), n1 = __builtin_fminf(pz[0], pz[1]), x2 = __builtin_fmaxf(pz[12], pz[13]), n2 = __builtin_fminf(pz[12], pz[13]);
#pragma unroll
    for (int e = 2; e < 12; e += 2) { x1 = max3(x1, pz[e], pz[e + 1]); n1 = min3(n1, pz[e], pz[e + 1]); }
    x2 = max3(x2, pz[14], pz[15]); n2 = min3(n2, pz[14], pz[15]);
    float lo, hi, lo2, hi2;
    xhalf(x1, lo, hi); xhalf(x2, lo2, hi2); mx = max3(lo, hi, lo2);
    xhalf(n1, lo, hi); xhalf(n2, lo2, hi2); mn = min3(lo, hi, lo2);
    return pz;
  };
  if (!ytype) {
    const int ub = line1 * 4 + 2 * n;
    ffn(c00, rs0, t0, ub); ffn(c01, rs1, t1, ub); ffn(c10, rs0, t0, ub + 1); ffn(c11, rs1, t1, ub + 1);
  } else {
    auto qout = [&](const f32x16& a, float rs, int tile, int c) {
      float mx, mn;
      const f32x16 z = prephi(a, rs, lb + L_WPQ + c * 1568 + wpl, mx, mn);
      u32x4 o0, o1;
#pragma unroll
      for (int e = 0; e < 4; ++e) { o0[e] = pk2(z[2 * e], z[2 * e + 1]); o1[e] = pk2(z[8 + 2 * e], z[9 + 2 * e]); }
      const u32 mm = pk2(mx, mn);
      o1[2] = h ? mm : o1[2]; o1[3] = h ? mm : o1[3];
      const u32 so = l * 16 + WS_PQ + ((panel * 4 + tile) * 16 + qg * 4 + c) * 2048;
      st_wt(rsw, so, o0); st_wt(rsw, so + 1024, o1);
    };
    qout(c00, rs0, t0, 2 * n); qout(c01, rs1, t1, 2 * n); qout(c10, rs0, t0, 2 * n + 1); qout(c11, rs1, t1, 2 * n + 1);
  }
  STAMP(3);
  {
    float mx, mn;
    f32x16 pz = prephi(ck, rs0, lb + L_WPK + wpl, mx, mn);
    mx *= -LOG2E; mn *= LOG2E;
    f32x2 ep[8], en[8], sp = {}, sn = {};
#pragma unroll
    for (int e = 0; e < 8; ++e) {
      const f32x2 v = {pz[2 * e], pz[2 * e + 1]};
      const f32x2 ap = v * LOG2E + mx, an = v * -LOG2E + mn;
      ep[e] = f32x2{ex2(ap[0]), ex2(ap[1])}; en[e] = f32x2{ex2(an[0]), ex2(an[1])};
      if (e >= 6) { ep[e] = h ? f32x2{} : ep[e]; en[e] = h ? f32x2{} : en[e]; }
      sp += ep[e]; sn += en[e];
    }
    float lo, hi;
    xhalf(sp[0] + sp[1], lo, hi); const float ip = __builtin_amdgcn_rcpf(lo + hi);
    xhalf(sn[0] + sn[1], lo, hi); const float in = __builtin_amdgcn_rcpf(lo + hi);
    const u32 fr = (r & 5) | ((r & 2) << 2) | ((r & 8) >> 2);
    const u32 pb = lb + L_PHI + m * L_SLOT + (32 * n + r) * 128;
#pragma unroll
    for (int g = 0; g < 4; ++g) {
      const f32x2 p0 = ep[2 * g] * ip, p1 = ep[2 * g + 1] * ip, n0 = en[2 * g] * in, n1 = en[2 * g + 1] * in;
      st64(pb + 8 * ((2 * g + h) ^ fr), u32x2{pk2(p0[0], p0[1]), pk2(p1[0], p1[1])});
      st64(pb + 8 * ((8 + 2 * g + h) ^ fr), u32x2{pk2(n0[0], n0[1]), pk2(n1[0], n1[1])});
    }
    u32x4 vb[2];
#pragma unroll
    for (int g = 0; g < 4; ++g) {
      const f32x4 rv = *(LAS f32x4*)(lb + L_ST + (32 * t0 + 8 * g + 4 * h) * 4);
      u32 a = pk2(cv[4 * g] * rv[0], cv[4 * g + 1] * rv[1]), b = pk2(cv[4 * g + 2] * rv[2], cv[4 * g + 3] * rv[3]);
      const u32 alt = r == 28 ? 0x3f803f80u : 0u;
      vb[g >> 1][2 * (g & 1)] = r < 28 ? a : alt; vb[g >> 1][2 * (g & 1) + 1] = r < 28 ? b : alt;
    }
    st128(lb + L_VF + t0 * 2048 + l * 16, vb[0]); st128(lb + L_VF + t0 * 2048 + 1024 + l * 16, vb[1]);
  }
  asm volatile("s_waitcnt vmcnt(0) lgkmcnt(0)" ::: "memory");
  if (lds_inc(lb + C_A) == 3u && l == 0) __hip_atomic_store((GAS u32*)(ws + WS_FH1 + (panel * 16 + j) * 128), tag, RLX_AGENT);
  STAMP(4);
  const int w = wid;
  if (w < 2) {
    lds_wait(lb + C_A, 4u);
    const u32 fq = ((q >> 1) << 3) | (h << 2) | (q & 1);
    const u32 sl = 8 * w + 4 * blk + p;
    const u32 b0 = lb + L_PHI + (4 * h + q) * 128 + 8 * (sl ^ fq);
    const u32 b1 = lb + L_PHI + (8 + 4 * h + q) * 128 + 8 * (sl ^ fq ^ 2);
    f32x16 acc = {};
#pragma unroll
    for (int s = 0; s < 8; ++s) {
      const u32 o = (s & 3) * 2048 + (s >> 2) * L_SLOT;
      acc = mfma(ldtr2(b0 + o, b1 + o), ld128(lb + L_VF + s * 1024 + l * 16), acc);
    }
#pragma unroll
    for (int g = 0; g < 4; ++g)
      st_wt(rsw, l * 16 + WS_KVP + ((j * 2 + w) * 16 + panel) * 4096 + g * 1024, __builtin_bit_cast(u32x4, f32x4{acc[4 * g], acc[4 * g + 1], acc[4 * g + 2], acc[4 * g + 3]}));
    lds_sig(lb + C_KV + 4 * w); lds_sig(lb + C_KV + 8 + 4 * w);
    WAITV(0);
    if (l == 0) __hip_atomic_store((GAS u32*)(ws + WS_FKV + ((j * 2 + w) * 16 + panel) * 128), tag, RLX_AGENT);
  }
  STAMP(5);
  if (reducer) {
    const int hf = xcd * 4 + idx;
    wait_flags(ws + WS_FKV + (hf * 16 + (l & 15)) * 128, tag);
    STAMP(6);
    f32x4 v[16];
#pragma unroll
    for (int i = 0; i < 16; ++i) v[i] = __builtin_bit_cast(f32x4, __builtin_amdgcn_raw_buffer_load_b128(rsw, l * 16, WS_KVP + (hf * 16 + i) * 4096 + w * 1024, 16));
#pragma unroll
    for (int o = 8; o; o >>= 1)
#pragma unroll
      for (int i = 0; i < o; ++i) v[i] += v[i + o];
    u64 val = (u64)pk2(v[0][0], v[0][1]) | ((u64)pk2(v[0][2], v[0][3]) << 32);
    if (r == 29) val = (w == 3 && h) ? 0ull : 0x3f803f803f803f80ull;
    __hip_atomic_store((GAS u64*)(ws + WS_KVF + hf * 2048 + (w >> 1) * 1024 + l * 16 + (w & 1) * 8), val, RLX_AGENT);
    WAITV(0);
    if (lds_inc(lb + C_R) == 3u && l == 0) __hip_atomic_store((GAS u32*)(ws + WS_FR + hf * 128), tag, RLX_AGENT);
    STAMP(7);
    return;
  }
  const u32x4 srdD = mksrd(wd, F_ * D_ * 2), srdO = mksrd(wo, D_ * D_ * 2);
  const u32 vw0 = (l >> 3) * 896 + ct * 128 + lc;
  const int rotE = (ct + 3 * (ti & 1)) % 6, rotL = (ct + 2 * (ti & 3)) & 7;
  auto blk2 = [&](int z) {
    if (z < 6) { int e = z + rotE; e = e >= 6 ? e - 6 : e; return 2 * (2 + 6 * w + 2 * (e >> 1)) + (e & 1); }
    const int e = (z - 6 + rotL) & 7;
    return 2 * (e < 6 ? 3 + 6 * w + 2 * (e >> 1) : (w < 2 ? w : w + 24)) + (e & 1);
  };
  auto issueW2 = [&](int z) {
    const u32 base = lb + thome(z % 5) + w * 4096;
    if (z < 14) dma4(srdD, vw0, vw0 + 6144, vw0 + 2 * 6144, vw0 + 3 * 6144, blk2(z) * (32 * 896), base);
    else dma4(srdO, vw0, vw0 + 6144, vw0 + 2 * 6144, vw0 + 3 * 6144, (4 * w + z - 14) * (28 * 896), base);
  };
  lds_wait4(lb + C_P1);
  issueW2(0); issueW2(1); issueW2(2);
  wait_flags(ws + WS_FH1 + ((ti >> 1) * 16 + 2 + 3 * w + ((l & 3) < 2 ? (l & 3) : 2)) * 128, tag);
  STAMP(6);
  const u32x4 srdH = mksrd(ws + WS_H, 64 * 112 * 1024);
  const u32 voffH = l * 16;
  u32x4 hb[14][4];
  auto issueH = [&](int z) {
    const u32 s0 = (2 * ti * 112 + 2 * blk2(z)) * 1024, s1 = s0 + 112 * 1024;
    asm volatile("buffer_load_dwordx4 %0, %4, %5, %6 offen sc1\n\tbuffer_load_dwordx4 %1, %4, %5, %6 offen offset:1024 sc1\n\t"
                 "buffer_load_dwordx4 %2, %4, %5, %7 offen sc1\n\tbuffer_load_dwordx4 %3, %4, %5, %7 offen offset:1024 sc1"
                 : "=&v"(hb[z][0]), "=&v"(hb[z][1]), "=&v"(hb[z][2]), "=&v"(hb[z][3]) : "v"(voffH), "s"(srdH), "s"(s0), "s"(s1) : "memory");
  };
#pragma unroll
  for (int z = 0; z < 6; ++z) issueH(z);
  lds_wait4(lb + C_P2);
  issueW2(3); issueW2(4);
  const u32 aw0 = lb + w * 4096 + (4 * h + q) * 128 + ((32 * blk + 8 * p) ^ rx);
  f32x16 acc[2][2] = {};
  auto stage = [&](int z, const u32x4 (&B)[4]) {
    u32x4 A[2][2];
    u32 s0 = aw0 + thome(z % 5); PIN(s0);
    u32 s1 = s0 ^ 64; PIN(s1);
#pragma unroll
    for (int s = 0; s < 2; ++s) {
      A[0][s] = ldtr2(s0 + s * 2048, s0 + s * 2048 + 1024);
      A[1][s] = ldtr2(s1 + s * 2048, s1 + s * 2048 + 1024);
    }
    if (z + 5 < 18) {
      asm volatile("s_waitcnt lgkmcnt(0)" : "+v"(A[0][0]), "+v"(A[0][1]), "+v"(A[1][0]), "+v"(A[1][1])::"memory");
      issueW2(z + 5);
    }
#pragma unroll
    for (int s = 0; s < 2; ++s)
#pragma unroll
      for (int a = 0; a < 2; ++a)
#pragma unroll
        for (int b = 0; b < 2; ++b) { acc[a][b] = mfma(A[a][s], B[2 * b + s], acc[a][b]); PIN(acc[a][b]); }
    SBAR();
  };
#define WAITH(z, n) asm volatile("s_waitcnt vmcnt(%4)" : "+v"(hb[z][0]), "+v"(hb[z][1]), "+v"(hb[z][2]), "+v"(hb[z][3]) : "i"(n) : "memory")
  WAITH(0, 28); stage(0, hb[0]);
  WAITH(1, 28); stage(1, hb[1]);
  WAITH(2, 28); stage(2, hb[2]);
  WAITH(3, 16); stage(3, hb[3]);
  const char* f2 = ws + WS_FH2 + ((ti >> 1) * 16 + ((l & 3) < 3 ? 2 + 3 * w + (l & 3) : (w < 2 ? w : w + 12))) * 128;
  u32 fl2;
  asm volatile("global_load_dword %0, %1, off sc1" : "=&v"(fl2) : "v"(f2) : "memory");
  WAITH(4, 17); stage(4, hb[4]);
  asm volatile("s_waitcnt vmcnt(4)" : "+v"(fl2)::"memory");
  if (!ALL(fl2 == tag)) wait_flags(f2, tag);
  STAMP(7);
#pragma unroll
  for (int z = 6; z < 14; ++z) issueH(z);
  WAITH(5, 36); stage(5, hb[5]);
#pragma unroll
  for (int z = 6; z < 10; ++z) { WAITH(z, 32); stage(z, hb[z]); }
#pragma unroll
  for (int z = 10; z < 14; ++z) { WAITH(z, 16); stage(z, hb[z]); }
  STAMP(8);
  int tz = TID; PIN(tz);
  const size_t orow = (size_t)(ti * 64 + (w >> 1) * 32 + (tz & 31)) * D_ + ct * 64 + (w & 1) * 32 + ((tz >> 3) & 4);
  u32x2 xr[4];
#pragma unroll
  for (int g = 0; g < 4; ++g) xr[g] = *(const u32x2*)(x + orow + 8 * g);
  u32 ao = lb + (w < 3 ? w * 16384 : L_WPQ) + (tz & 63) * 16; PIN(ao);
  BAR();
  STAMP(9);
#pragma unroll
  for (int z = 14; z < 18; ++z) {
    WAITV((17 - z) * 4 + 4);
    u32x4 B[4];
#pragma unroll
    for (int i = 0; i < 4; ++i) B[i] = ld128(ao + (z - 14) * 4096 + i * 1024);
    stage(z, B);
  }
  if (BID == 32 && TID == 0) __hip_atomic_store((GAS u32*)(ws + WS_EP), tag, RLX_AGENT);
  f32x16 mine = {};
#pragma unroll
  for (int t = 0; t < 4; ++t) {
    const f32x16& a = acc[t & 1][t >> 1];
    if (t == w) mine = a;
    else {
#pragma unroll
      for (int g = 0; g < 4; ++g)
        *(LAS f32x4*)(lb + thome(t + 1) + w * 4096 + g * 1024 + l * 16) = f32x4{a[4 * g], a[4 * g + 1], a[4 * g + 2], a[4 * g + 3]};
    }
  }
  BAR();
  const u32 rb = lb + (w < 2 ? thome(1) : thome(3)) + (w & 1) * 16384 + l * 16;
#pragma unroll
  for (int o = 1; o < 4; ++o) {
    const u32 src = rb + ((w + o) & 3) * 4096;
#pragma unroll
    for (int g = 0; g < 4; ++g) {
      const f32x4 v = *(LAS f32x4*)(src + g * 1024);
#pragma unroll
      for (int e = 0; e < 4; ++e) mine[4 * g + e] += v[e];
    }
  }
#pragma unroll
  for (int g = 0; g < 4; ++g)
    *(u32x2*)(out + orow + 8 * g) = u32x2{pk2(mine[4 * g] + lo16(xr[g][0]), mine[4 * g + 1] + hi16(xr[g][0])),
                                          pk2(mine[4 * g + 2] + lo16(xr[g][1]), mine[4 * g + 3] + hi16(xr[g][1]))};
  STAMP(10);
}

#if defined(__HIP_DEVICE_COMPILE__)
#pragma clang attribute pop
#endif

extern "C" void kernel_launch(void* const* d_in, const int* in_sizes, int n_in, void* d_out, int out_size, void* d_ws, size_t ws_size,
                              hipStream_t stream) {
  fused<<<256, 512, 0, stream>>>((const u16*)d_in[0], (const u16*)d_in[1], (const u16*)d_in[2], (const u16*)d_in[3], (const u16*)d_in[5],
                                 (const u16*)d_in[6], (const u16*)d_in[7], (char*)d_ws, (u16*)d_out);
}
```

```cpp
#include <hip/hip_runtime.h>
#pragma clang diagnostic ignored "-Wint-to-pointer-cast"

#if defined(__HIP_DEVICE_COMPILE__)
#pragma clang attribute push(__attribute__((target("no-packed-fp32-ops"))), apply_to = function)
#endif
#define TID ((int)__builtin_amdgcn_workitem_id_x())
#define BID ((int)__builtin_amdgcn_workgroup_id_x())
#define ALL(c) (__builtin_amdgcn_ballot_w64(!(c)) == 0)
#define F2U(x) __builtin_bit_cast(u32, (float)(x))
#define U2F(x) __builtin_bit_cast(float, (u32)(x))

typedef unsigned short u16;
typedef unsigned int u32;
typedef unsigned long long u64;
typedef __attribute__((ext_vector_type(8))) __bf16 bf16x8;
typedef __attribute__((ext_vector_type(2))) __bf16 bf16x2;
typedef __attribute__((ext_vector_type(4))) short s16x4;
typedef __attribute__((ext_vector_type(16))) float f32x16;
typedef __attribute__((ext_vector_type(4))) float f32x4;
typedef __attribute__((ext_vector_type(4))) u32 u32x4;
typedef __attribute__((ext_vector_type(2))) u32 u32x2;
#define LAS __attribute__((address_space(3)))
#define GAS __attribute__((address_space(1)))
#define DI __device__ __forceinline__
#define SBAR() __builtin_amdgcn_sched_barrier(0)
#define PIN(x) asm volatile("" : "+v"(x))
#define RLX_AGENT __ATOMIC_RELAXED, __HIP_MEMORY_SCOPE_AGENT

constexpr int S_ = 2048, D_ = 448, F_ = 1792;
constexpr u32 WS_H = 0;
constexpr u32 WS_PQ = WS_H + 64 * 144 * 1024;
constexpr u32 WS_KVP = WS_PQ + 64 * 16 * 4096;
constexpr u32 WS_CS = WS_KVP + 32 * 16 * 2048;
constexpr u32 WS_ST = WS_CS + 16 * 10 * 2048;
constexpr u32 WS_FH = WS_ST + 2048 * 8;
constexpr u32 WS_FQ = WS_FH + 256 * 128;
constexpr u32 WS_FKV = WS_FQ + 128 * 128;
constexpr u32 WS_FO = WS_FKV + 512 * 128;
constexpr u32 WS_EP = WS_FO + 257 * 128;
constexpr u32 WS_END = WS_EP + 512;

DI f32x16 mfma(u32x4 a, u32x4 b, f32x16 c) {
  return __builtin_amdgcn_mfma_f32_32x32x16_bf16(__builtin_bit_cast(bf16x8, a), __builtin_bit_cast(bf16x8, b), c, 0, 0, 0);
}
DI u32 pk2(float lo, float hi) { bf16x2 p; p[0] = (__bf16)lo; p[1] = (__bf16)hi; return __builtin_bit_cast(u32, p); }
DI float rbf(float f) { return (float)(__bf16)f; }
DI float lo16(u32 u) { return U2F(u << 16); }
DI float hi16(u32 u) { return U2F(u & 0xffff0000u); }
DI float dot2(u32 a, u32 b, float c) {
  return __builtin_amdgcn_fdot2_f32_bf16(__builtin_bit_cast(bf16x2, a), __builtin_bit_cast(bf16x2, b), c, false);
}
DI u32x2 ldtr(u32 addr) { return __builtin_bit_cast(u32x2, __builtin_amdgcn_ds_read_tr16_b64_v4i16((LAS s16x4*)addr)); }
DI u32x4 ldtr2(u32 a0, u32 a1) { u32x2 a = ldtr(a0), b = ldtr(a1); return u32x4{a[0], a[1], b[0], b[1]}; }
DI u32x4 ld128(u32 addr) { return *(LAS u32x4*)addr; }
DI void st128(u32 addr, u32x4 v) { *(LAS u32x4*)addr = v; }
DI void st64(u32 addr, u32x2 v) { *(LAS u32x2*)addr = v; }
DI u32x4 mksrd(const void* p, u32 bytes) {
  u64 a = (u64)p;
  return u32x4{(u32)a, (u32)(a >> 32) & 0xffffu, bytes, 0x00020000u};
}
DI void dma1(u32x4 srd, u32 v0, u32 soff, u32 lds) {
  u32 k;
  asm volatile("s_mov_b32 %0, m0\n\ts_mov_b32 m0, %4\n\ts_nop 0\n\tbuffer_load_dwordx4 %1, %2, %3 offen lds\n\ts_mov_b32 m0, %0"
               : "=&s"(k) : "v"(v0), "s"(srd), "s"(soff), "s"(lds) : "memory");
}
DI void dma4(u32x4 srd, u32 v0, u32 v1, u32 v2, u32 v3, u32 soff, u32 lds) {
  u32 k;
  asm volatile("s_mov_b32 %0, m0\n\ts_mov_b32 m0, %7\n\ts_nop 0\n\tbuffer_load_dwordx4 %1, %5, %6 offen lds\n\tbuffer_load_dwordx4 %2, %5, %6 offen offset:1024 lds\n\t"
               "buffer_load_dwordx4 %3, %5, %6 offen offset:2048 lds\n\tbuffer_load_dwordx4 %4, %5, %6 offen offset:3072 lds\n\ts_mov_b32 m0, %0"
               : "=&s"(k) : "v"(v0), "v"(v1), "v"(v2), "v"(v3), "s"(srd), "s"(soff), "s"(lds) : "memory");
}
DI void dmaw(u32x4 srd, u32 v0, u32 lds) {
  u32 k;
  asm volatile("s_mov_b32 %0, m0\n\ts_mov_b32 m0, %3\n\ts_nop 0\n\tbuffer_load_dword %1, %2, 0 offen lds\n\ts_mov_b32 m0, %0"
               : "=&s"(k) : "v"(v0), "s"(srd), "s"(lds) : "memory");
}
template <int OFF> DI void dmap(u32x4 srd, u32 v0, u32 soff, u32 lds) {
  u32 k;
  asm volatile("s_mov_b32 %0, m0\n\ts_mov_b32 m0, %4\n\ts_nop 0\n\tbuffer_load_dwordx4 %1, %2, %3 offen offset:%5 lds\n\ts_mov_b32 m0, %0"
               : "=&s"(k) : "v"(v0), "s"(srd), "s"(soff), "s"(lds), "i"(OFF) : "memory");
}
template <int OFF> DI void ldh(u32x4& d, u32x4 srd, u32 v0, u32 soff) {
  asm volatile("buffer_load_dwordx4 %0, %1, %2, %3 offen offset:%4 sc1" : "=&v"(d) : "v"(v0), "s"(srd), "s"(soff), "i"(OFF) : "memory");
}
DI u64 add64(u64 a, u32 b) {
  u32 lo, hi;
  asm("v_add_co_u32 %0, vcc, %4, %2\n\ts_nop 1\n\tv_addc_co_u32 %1, vcc, 0, %3, vcc" : "=&v"(lo), "=v"(hi) : "v"((u32)a), "v"((u32)(a >> 32)), "s"(b) : "vcc");
  return (u64)lo | ((u64)hi << 32);
}
DI void gdma4(u64 a0, u64 a1, u64 a2, u64 a3, u32 lds) {
  u32 k;
  asm volatile("s_mov_b32 %0, m0\n\ts_mov_b32 m0, %5\n\ts_nop 0\n\tglobal_load_lds_dwordx4 %1, off\n\tglobal_load_lds_dwordx4 %2, off offset:1024\n\t"
               "global_load_lds_dwordx4 %3, off offset:2048\n\tglobal_load_lds_dwordx4 %4, off offset:3072\n\ts_mov_b32 m0, %0"
               : "=&s"(k) : "v"(a0), "v"(a1), "v"(a2), "v"(a3), "s"(lds) : "memory");
}
#define WAITV(n) asm volatile("s_waitcnt vmcnt(%0)" ::"i"(n) : "memory")
#define BAR() do { asm volatile("" ::: "memory"); __builtin_amdgcn_s_waitcnt(0xC07F); __builtin_amdgcn_s_barrier(); asm volatile("" ::: "memory"); SBAR(); } while (0)
DI void xhalf(float v, float& lo, float& hi) {
  auto r = __builtin_amdgcn_permlane32_swap(F2U(v), F2U(v), false, false);
  lo = U2F(r[0]); hi = U2F(r[1]);
}
DI float xhalf_hi(float v) { float lo, hi; xhalf(v, lo, hi); return hi; }
constexpr int SPIN_CAP = 1 << 20;
DI void wait_flags(const char* p, u32 tag) {
#pragma unroll 1
  for (int spin = 0; spin < SPIN_CAP; ++spin) {
    const u32 v = __hip_atomic_load((GAS u32*)p, RLX_AGENT);
    if (ALL(v == tag)) break;
    __builtin_amdgcn_s_sleep(2);
  }
  __builtin_amdgcn_fence(__ATOMIC_ACQUIRE, "wavefront");
}
DI u32 lds_inc(u32 addr) {
  u32 o = 0;
  if ((TID & 63) == 0) o = __hip_atomic_fetch_add((LAS u32*)addr, 1u, __ATOMIC_RELAXED, __HIP_MEMORY_SCOPE_WORKGROUP);
  return __builtin_amdgcn_readfirstlane(o);
}
template <typename R> DI void st_wt(R rs, u32 off, u32x4 v) { __builtin_amdgcn_raw_buffer_store_b128(v, rs, off, 0, 16); }
DI void lds_wait(u32 addr, u32 n) {
#pragma unroll 1
  for (int spin = 0; spin < SPIN_CAP; ++spin) {
    if (__builtin_amdgcn_readfirstlane(__hip_atomic_load((LAS u32*)addr, __ATOMIC_RELAXED, __HIP_MEMORY_SCOPE_WORKGROUP)) >= n) break;
    __builtin_amdgcn_s_sleep(1);
  }
  asm volatile("" ::: "memory");
}
DI void lds_sig(u32 addr) {
  asm volatile("" ::: "memory");
  *(volatile LAS u32*)addr = 1u;
  asm volatile("" ::: "memory");
}
DI void lds_wait4(u32 addr) {
#pragma unroll 1
  for (int spin = 0; spin < SPIN_CAP; ++spin) {
    const u32x4 v = *(volatile LAS u32x4*)addr;
    if (__builtin_amdgcn_readfirstlane(v[0] & v[1] & v[2] & v[3])) break;
    __builtin_amdgcn_s_sleep(1);
  }
  asm volatile("" ::: "memory");
}
DI float max3(float a, float b, float c) { float d; asm("v_max3_f32 %0, %1, %2, %3" : "=v"(d) : "v"(a), "v"(b), "v"(c)); return d; }
DI float min3(float a, float b, float c) { float d; asm("v_min3_f32 %0, %1, %2, %3" : "=v"(d) : "v"(a), "v"(b), "v"(c)); return d; }
DI float ex2(float a) { return __builtin_amdgcn_exp2f(a); }
constexpr float LOG2E = 1.4426950408889634f;

constexpr u32 L_SLOT = 57344, L_WH = 20480, L_X = 40960, L_PHI = 114688, L_FIX = 57344, L_VF = 131072, L_PK = 139264, L_WPQ = 147456,
              L_WPK = L_WPQ + 7168, L_ST = L_WPK + 2048, L_SD = L_ST + 1024, L_CTL = L_SD + 256, L_JUNK = L_CTL + 256, L_FIXK = L_JUNK + 256;
constexpr u32 C_A = L_CTL + 4, C_Y = L_CTL + 12, C_KV = L_CTL + 16, C_PK = L_CTL + 32, C_PH = L_CTL + 48, C_S = L_CTL + 80, C_O = L_CTL + 96, C_K2 = L_CTL + 128;
DI constexpr u32 whome(int z) { return (z < 6 ? z % 3 + 4 : (z - 6) % 5) * 16384; }
constexpr int ZO = 12;
constexpr int refill(int z) { return z <= 10 ? z + 7 : -1; }
constexpr int hmax(int z) { return z < 10 ? z + 4 : (z < ZO ? 13 : (13 + 2 * (z - ZO + 1) > 17 ? 17 : 13 + 2 * (z - ZO + 1))); }
constexpr int p2_wait(int z) {
  int posH[18] = {}, posW[18] = {}, n = 0;
  for (int i = 0; i < 18; ++i) posW[i] = -1;
  for (int it = -1; it < z; ++it) {
    for (int k = it < 0 ? 0 : hmax(it - 1) + 1; k <= hmax(it); ++k) posH[k] = n++;
    if (it >= 0 && refill(it) >= 0) posW[refill(it)] = n++;
  }
  int need = posH[z];
  if (z + 3 < 18 && posW[z + 3] > need) need = posW[z + 3];
  return 4 * (n - 1 - need);
}
constexpr int CODE_LINES = 220;

__global__ __launch_bounds__(512, 2) void fused(const u16* __restrict__ x, const u16* __restrict__ wqkv, const u16* __restrict__ wphi,
                                                const u16* __restrict__ wo, const u16* __restrict__ wg, const u16* __restrict__ wu,
                                                const u16* __restrict__ wd, char* __restrict__ ws, u16* __restrict__ out) {
  __shared__ __attribute__((aligned(16))) char smem[163840];
  u64 pc0; asm volatile("s_getpc_b64 %0" : "=s"(pc0));
  const u32 lb = (u32)(size_t)(LAS char*)smem;
  const int wid = __builtin_amdgcn_readfirstlane(TID >> 6);
  const int l = TID & 63;

  const int xcd = BID & 7, idx = BID >> 3;
  const int panel = (xcd >> 2) * 8 + (idx & 7);
  const int j = (xcd & 3) * 4 + (idx >> 3);
  const bool ytype = (j & 1) == (j < 8);
  const int sec = (j >> 1) & 1, qp = j >> 1;
  const int line0 = 7 * (j >> 2) + (ytype ? 4 + sec : 2 * sec), lineh = 7 * (j >> 2) + 6;
  const int koff = (j & 1) * 8;
  const int rot = (int)((u32)(panel + 2 * j) % 7u);
  const int w = wid & 3;
  const u32 vx0 = (l >> 3) * 896 + 16 * ((l & 7) ^ (l >> 4)), vx1 = (l >> 3) * 896 + 16 * ((l & 7) ^ (4 + (l >> 4))) + 6144;
  const u32 xrow0 = (panel * 128 + w * 32) * 896;
  asm volatile("" ::"s"(x), "s"(wqkv), "s"(wphi), "s"(wo), "s"(wg), "s"(wu), "s"(wd), "s"(ws), "s"(out));
  const u32x4 srdQ = mksrd(wqkv, D_ * 3 * D_ * 2), srdX = mksrd(x, S_ * D_ * 2);
  auto ksof = [&](int s) { const int ks = s + 2 * rot; return ks >= 14 ? ks - 14 : ks; };
  auto issueX = [&](int i) { dma4(srdX, vx0, vx1, vx0 + 2 * 6144, vx1 + 2 * 6144, xrow0 + ksof(2 * i) * 64, lb + (u32)(i & 1) * L_SLOT + L_X + w * 4096); };
  u32 ep; u32x4 xs[7];
  if (wid < 4) {
    issueX(0);
    if (wid == 0) {
      const u32x4* src = (const u32x4*)(x + (panel * 128 + j * 8 + (l >> 3)) * D_) + (l & 7);
#pragma unroll
      for (int i = 0; i < 7; ++i) xs[i] = src[i * 8];
      ep = __hip_atomic_load((GAS u32*)(ws + WS_EP), RLX_AGENT);
    } else if (wid == 2)
      dmaw(mksrd((const void*)(pc0 & ~127ull), CODE_LINES * 128), ((idx & 3) * 64 + l) * 128, lb + L_JUNK);
  }
  SBAR();
#define MM(c, a, b) do { c = mfma(a, b, c); PIN(c); SBAR(); } while (0)
#define RW(Wr, a, no) do { Wr = ldtr2((a) + (no), (a) + (no) + 512); } while (0)
  u32 tagc;
  if (__builtin_expect(wid >= 4, 1)) {
    const u32 lc = (16 * (l & 7)) ^ (64 * ((l >> 4) & 1));
    const bool isq = ytype && w == 3, mix = ytype && w == 2;
    const bool isu = mix ? lc >= 64 : (w & 1);
    const u32 wst = isq ? 2688 : 3584;
    const u32 col = isq ? (qp == 7 ? 768 : 112 * qp) + lc : (mix ? lineh * 128 + sec * 64 + ((lc & 63) ^ (isu ? 32 : 0)) : (line0 + (w >> 1)) * 128 + (lc ^ (isu ? 32 : 0)));
    const u64 pw0 = (u64)(isq ? wqkv : (isu ? wu : wg)) + (l >> 3) * wst + col;
    const u32 vkv = (l >> 3) * 2688 + 896 + 56 * j - koff + (lc < 64 ? lc : lc + 896 - 64);
    auto wbase = [&](int s) { return lb + (u32)((s >> 1) & 1) * L_SLOT + (u32)(s & 1) * L_WH; };
    auto issueWw = [&](int s) {
      const u32 o = 32 * ksof(s) * wst, d = 8 * wst - 1024;
      gdma4(add64(pw0, o), add64(pw0, o + d), add64(pw0, o + 2 * d), add64(pw0, o + 3 * d), wbase(s) + w * 4096);
    };
    auto issueWk = [&](int s) { dma1(srdQ, vkv, (32 * ksof(s) + 8 * w) * 2688, wbase(s) + 16384 + w * 1024); };
    issueWw(0); issueWk(0); issueWw(1); issueWk(1);
    SBAR();
    int lq = l; PIN(lq);
    const int blk = (lq >> 4) & 1, q = (lq & 15) >> 2, p = lq & 3, h = lq >> 5, r = lq & 31;
    const u32 rowpart = (8 * h + q) * 128, rx = 64 * ((q >> 1) & 1);
    const auto rsw = __builtin_amdgcn_make_buffer_rsrc((void*)ws, 0, WS_END, 0x00020000);
    auto uaddr = [&](int n, int u) {
      u32 sub, cb;
      if (!ytype) { sub = 2 * n + blk; cb = (32 * u + 8 * p) ^ (32 * blk); }
      else if (u < 2) { sub = blk; cb = (32 * (2 * n + u) + 8 * p) ^ (32 * blk); }
      else if (u == 2) { sub = 2; cb = 64 * blk + ((32 * n + 8 * p) ^ (32 * blk)); }
    else { sub = 3; cb = ((qp == 7 ? 16 : 0) + 56 * n + 32 * blk + 8 * p) & 127; }
      return lb + sub * 4096 + rowpart + (cb ^ rx);
    };
    const u32 k_addr = lb + 16384 + rowpart + (((koff + 32 * blk + 8 * p) & 63) ^ rx);
    const int redl = (xcd & 2) ? 2 : 4;
    const bool reducer = (idx >> 2) == redl;
    const int ti = xcd * 4 + (idx & 3), ct = (idx >> 2) - ((idx >> 2) > redl);

    const bool prod = panel < 14;
    const int pn = panel >= 7;
    const u32 csb = WS_CS + j * 20480;
    u32 tag;
    auto pubcs = [&](const u32x4 (&f)[4], int unit) {
      const u32x4 ones = {0x3f803f80u, 0x3f803f80u, 0x3f803f80u, 0x3f803f80u};
      f32x16 c = {};
      __builtin_amdgcn_s_setprio(3);
#pragma unroll
      for (int k = 0; k < 4; ++k) c = mfma(ones, f[k], c);
      PIN(c);
      __builtin_amdgcn_s_setprio(0);
      const float s = c[0], s0 = rbf(s);
      if (l < 32 || rot == 6)
        __builtin_amdgcn_raw_buffer_store_b64(u32x2{h ? 0u : pk2(s0, s - s0), tag}, rsw, r * 16 + h * 8, csb + unit * 2048 + (rot >> 1) * 512 + (rot & 1) * 8, 16);
    };
    const u32 ca0 = uaddr(pn, w), ca1 = k_addr ^ (64 * pn);
    u32x4 f0[4], f1[4];
    auto rdcs = [&](int hh) {
      if (prod) {
#pragma unroll
        for (int k = 0; k < 2; ++k) { RW(f0[2 * hh + k], ca0, hh * L_WH + k * 2048); if (w == 0) RW(f1[2 * hh + k], ca1, hh * L_WH + k * 2048); }
      }
    };
#define BARN() do { asm volatile("" ::: "memory"); __builtin_amdgcn_s_barrier(); asm volatile("" ::: "memory"); SBAR(); } while (0)
    WAITV(5); BAR();
    rdcs(0);
    issueWw(2); issueWk(2); issueX(1);
    WAITV(9); BAR();
    rdcs(1);
    issueWw(3); issueWk(3); issueWw(4); issueWk(4);
    tag = __builtin_amdgcn_readfirstlane(*(LAS u32*)(lb + L_CTL));
    WAITV(10); BAR();
    issueX(2); issueWw(5);
    WAITV(13); BARN();
    issueWk(5); issueWw(6); issueWk(6);
    if (prod) pubcs(f0, 4 * pn + w);

    WAITV(10); BARN();
    issueX(3); issueWw(7);
    WAITV(13); BARN();
    issueWk(7); issueWw(8); issueWk(8);
    if (prod && w == 0) pubcs(f1, 8 + pn);

    WAITV(10); BARN();
    issueX(4); issueWw(9);
    WAITV(13); BARN();
    issueWk(9); issueWw(10); issueWk(10);
    {
      const u32x4 srdP = mksrd(wphi, 16 * 784 * 2);
      if (w < 2) dma1(srdP, l * 16, j * 1568 + w * 1024, lb + L_WPK + w * 1024);
      if (ytype) dma1(srdP, l * 16, qp * 3136 + w * 1024, lb + L_WPQ + w * 1024);
    }
    WAITV(10); BARN();
    issueX(5); issueWw(11);
    WAITV(13); BARN();
    issueWk(11); issueWw(12); issueWk(12);
    const u32 goff = h * 1024 + r * 16;
    const u32x4 srdS = mksrd(ws, WS_END);
    u32x4 g0[2], g1[2], g2[2]; u32x2 s2;
    auto gather = [&](u32x4 (&g)[2], int unit) {
      asm volatile("buffer_load_dwordx4 %0, %2, %3, %4 offen sc1\n\tbuffer_load_dwordx4 %1, %2, %3, %4 offen offset:512 sc1"
                   : "=&v"(g[0]), "=&v"(g[1]) : "v"(goff), "s"(srdS), "s"(csb + unit * 2048) : "memory");
    };
    auto gather_all = [&]() {
      gather(g0, w); gather(g1, 4 + w); gather(g2, 8 + (w & 1));
      asm volatile("buffer_load_dwordx2 %0, %1, %2, %3 offen sc1" : "=&v"(s2) : "v"(r * 8), "s"(srdS), "s"(WS_ST + (panel * 128 + w * 32) * 8) : "memory");
    };
#define WAITG(n) asm volatile("s_waitcnt vmcnt(%7)" : "+v"(g0[0]), "+v"(g0[1]), "+v"(g1[0]), "+v"(g1[1]), "+v"(g2[0]), "+v"(g2[1]), "+v"(s2) : "i"(n) : "memory")
    auto gbad = [&]() {
      u32 an = s2[1], orr = s2[1];
#pragma unroll
      for (int i = 0; i < 2; ++i) {
        an &= g0[i][1] & g0[i][3]; orr |= g0[i][1] | g0[i][3]; an &= g1[i][1] & g1[i][3]; orr |= g1[i][1] | g1[i][3];
        an &= g2[i][1] & g2[i][3]; orr |= g2[i][1] | g2[i][3];
      }
      return !ALL(an == tag && orr == tag);
    };
    const u32x4 srdD = mksrd(wd, reducer ? 0 : F_ * D_ * 2), srdO = mksrd(wo, D_ * D_ * 2);
    const u32 vw0 = (l >> 3) * 896 + ct * 128 + lc;
    const int rot2 = (2 * ct + ((ti & 3) * 7 >> 1)) % 14;
    auto blk2 = [&](int z) { const int e = z + rot2; return 14 * w + (e >= 14 ? e - 14 : e); };
    auto issueW2 = [&](int z) {
      const u32 base = lb + whome(z) + w * 4096;
      if (z < 14) dma4(srdD, vw0, vw0 + 6144, vw0 + 2 * 6144, vw0 + 3 * 6144, blk2(z) * (32 * 896), base);
      else dma4(srdO, vw0, vw0 + 6144, vw0 + 2 * 6144, vw0 + 3 * 6144, (4 * w + z - 14) * (28 * 896), base);
    };
    u32x4 R[4][4];
    auto issueR = [&](int k) {
      asm volatile("buffer_load_dwordx4 %0, %4, %8, %9 offen\n\tbuffer_load_dwordx4 %1, %5, %8, %9 offen offset:1024\n\t"
                   "buffer_load_dwordx4 %2, %6, %8, %9 offen offset:2048\n\tbuffer_load_dwordx4 %3, %7, %8, %9 offen offset:3072"
                   : "=&v"(R[k][0]), "=&v"(R[k][1]), "=&v"(R[k][2]), "=&v"(R[k][3])
                   : "v"(vw0), "v"(vw0 + 6144), "v"(vw0 + 2 * 6144), "v"(vw0 + 3 * 6144), "s"(srdD), "s"(blk2(3 + k) * (32 * 896)) : "memory");
    };
    gather_all();
    WAITV(10 + 7); BARN();
    issueX(6); issueWk(13); issueWw(13);
    WAITV(5 + 7 + 9); BARN();
    issueR(0);
    WAITG(9 + 4);
#pragma unroll 1
    for (int spin = 0; spin < SPIN_CAP && gbad(); ++spin) { __builtin_amdgcn_s_sleep(1); gather_all(); WAITG(0); }
    st128(lb + L_FIX + w * 1024 + l * 16, u32x4{g0[0][0], g0[0][2], g0[1][0], g0[1][2]});
    st128(lb + L_FIX + 4096 + w * 1024 + l * 16, u32x4{g1[0][0], g1[0][2], g1[1][0], g1[1][2]});
    st128(lb + L_FIXK + (w & 1) * 1024 + l * 16, u32x4{g2[0][0], g2[0][2], g2[1][0], g2[1][2]});
    const float rs0 = __builtin_amdgcn_rcpf(hi16(s2[0]));
    *(LAS u32*)(lb + L_ST + h * 512 + (32 * w + r) * 4) = h ? ((s2[0] & 0xffffu) ^ 0x8000u) * 0x10001u : F2U(rs0);
    *(LAS u16*)(lb + L_SD + (32 * w + r) * 2) = (u16)(s2[0] >> 16);
    issueR(1);
    WAITV(4 + 8);
    BAR();
    issueW2(0); issueW2(1); issueW2(2); WAITV(8 + 12);
    BAR();

    const u32 aw0 = lb + w * 4096 + (4 * h + q) * 128 + ((32 * blk + 8 * p) ^ rx ^ (64 * (w & 1)));
    u32x4 A[18][4];
    auto readA = [&](int z) {
      u32 s0 = aw0 + whome(z); PIN(s0);
      u32 s1 = s0 ^ 64; PIN(s1);
#pragma unroll
      for (int s = 0; s < 2; ++s) { A[z][s] = ldtr2(s0 + s * 2048, s0 + s * 2048 + 1024); A[z][2 + s] = ldtr2(s1 + s * 2048, s1 + s * 2048 + 1024); }
    };
#define WAITA(z) asm volatile("s_waitcnt lgkmcnt(0)" : "+v"(A[z][0]), "+v"(A[z][1]), "+v"(A[z][2]), "+v"(A[z][3])::"memory")
    issueR(2); issueR(3);
    if (w == 3 && !reducer) {
      const u32 a = ((ti & 3) * 112 + l) * 896 + ct * 128;
      dmaw(srdO, a, lb + L_JUNK); dmaw(srdO, a + 64 * 896, lb + L_JUNK);
    }
    const size_t orow = (size_t)(ti * 64 + (w >> 1) * 32 + r) * D_ + ct * 64 + (w & 1) * 32 + 4 * h;
    u32x2 xr[4];
#pragma unroll
    for (int g = 0; g < 4; ++g) xr[g] = *(const u32x2*)(x + orow + 8 * g);
    lds_wait(lb + C_PK + 4 * w, 1u);

    {
      const u32x4 p0 = ld128(lb + L_PK + w * 2048 + l * 16), p1 = ld128(lb + L_PK + w * 2048 + 1024 + l * 16);
      float mm, lo, hi;
      xhalf(U2F(p1[2]), lo, mm);
      const float sc = rs0 * LOG2E, mx = lo16(F2U(mm)) * -sc, mn = hi16(F2U(mm)) * sc;
      const float mx2 = h ? -__builtin_inff() : mx, mn2 = h ? -__builtin_inff() : mn;
      float ep[16], en[16], s0 = 0.f, s1 = 0.f, s2_ = 0.f, s3 = 0.f;
#pragma unroll
      for (int e = 0; e < 16; ++e) {
        const u32 u = e < 8 ? p0[e >> 1] : ((e >= 14 && h) ? F2U(mm) : p1[(e - 8) >> 1]);
        const float v = (e & 1) ? hi16(u) : lo16(u);
        ep[e] = ex2(v * sc + (e < 12 ? mx : mx2)); en[e] = ex2((e < 12 ? mn : mn2) - v * sc);
        if (e & 1) { s1 += ep[e]; s3 += en[e]; } else { s0 += ep[e]; s2_ += en[e]; }
      }
      xhalf(s0 + s1, lo, hi); const float ip = __builtin_amdgcn_rcpf(lo + hi) * rs0;
      xhalf(s2_ + s3, lo, hi); const float in = __builtin_amdgcn_rcpf(lo + hi) * rs0;
      const u32 fr = (r & 5) | ((r & 2) << 2) | ((r & 8) >> 2);
      const u32 pb = lb + L_PHI + (32 * w + r) * 128;
#pragma unroll
      for (int g = 0; g < 4; ++g) {
        st64(pb + 8 * ((2 * g + h) ^ fr), u32x2{pk2(ep[4 * g] * ip, ep[4 * g + 1] * ip), pk2(ep[4 * g + 2] * ip, ep[4 * g + 3] * ip)});
        st64(pb + 8 * ((8 + 2 * g + h) ^ fr), u32x2{pk2(en[4 * g] * in, en[4 * g + 1] * in), pk2(en[4 * g + 2] * in, en[4 * g + 3] * in)});
      }
    }
    asm volatile("s_waitcnt lgkmcnt(0)" ::: "memory");
    lds_sig(lb + C_PH + 4 * w);
    if (w < 2) {
      lds_wait4(lb + C_PH); lds_wait4(lb + C_PK);
      const u32 fq = ((q >> 1) << 3) | (h << 2) | (q & 1);
      const u32 sl = 8 * w + 4 * blk + p;
      const u32 b0 = lb + L_PHI + (4 * h + q) * 128 + 8 * (sl ^ fq);
      const u32 b1 = lb + L_PHI + (8 + 4 * h + q) * 128 + 8 * (sl ^ fq ^ 2);
      u32x4 fa[8], fb[8];
#pragma unroll
      for (int s = 0; s < 8; ++s) { fa[s] = ldtr2(b0 + s * 2048, b1 + s * 2048); fb[s] = ld128(lb + L_VF + s * 1024 + l * 16); }
      SBAR();
      f32x16 acc = {};
#pragma unroll
      for (int s = 0; s < 8; ++s) acc = mfma(fa[s], fb[s], acc);
#pragma unroll
      for (int g = 0; g < 2; ++g)
        st_wt(rsw, l * 16 + WS_KVP + ((j * 2 + w) * 16 + panel) * 2048 + g * 1024,
              u32x4{pk2(acc[8 * g], acc[8 * g + 1]), pk2(acc[8 * g + 2], acc[8 * g + 3]), pk2(acc[8 * g + 4], acc[8 * g + 5]), pk2(acc[8 * g + 6], acc[8 * g + 7])});
    }
    auto pubkv = [&]() { if (w < 2 && l == 0) __hip_atomic_store((GAS u32*)(ws + WS_FKV + ((j * 2 + w) * 16 + panel) * 128), tag, RLX_AGENT); };
    if (reducer) { WAITV(0); pubkv(); }

    if (!reducer) {
    if (w < 2) WAITV(2); else WAITV(0);
#pragma unroll
    for (int k = 0; k < 4; ++k) { PIN(R[k][0]); PIN(R[k][1]); PIN(R[k][2]); PIN(R[k][3]); }
    readA(0); readA(1); readA(2);
    WAITA(0); WAITA(1); WAITA(2);
    auto putR = [&](int k) {
#pragma unroll
      for (int i = 0; i < 4; ++i) st128(lb + whome(3 + k) + w * 4096 + i * 1024 + l * 16, R[k][i]);
    };
    putR(0); putR(1); putR(2);
    WAITV(0); pubkv();
    lds_sig(lb + C_KV + 4 * w);
    lds_wait4(lb + C_S);
    putR(3);
    lds_wait4(lb + C_KV);

    {
      wait_flags(ws + WS_FH + ((ti >> 1) * 16 + 4 * w + (l & 3)) * 128, tag);
    }

    const u32x4 srdH = mksrd(ws + WS_H, 64 * 144 * 1024);
    const u32 voffH = l * 16;
    u32x4 hb[18][4];
    const u32 hb0 = (2 * ti + (w >> 1)) * (144 * 1024), hb1 = (2 * ti + 1 - (w >> 1)) * (144 * 1024);
    auto hoff = [&](int z) { return (u32)(z < 14 ? blk2(z) : 56 + 4 * w + z - 14) * 2048u; };
    auto issueH = [&](int z) {
      const u32 s0 = hb0 + hoff(z), s1 = hb1 + hoff(z);
      asm volatile("buffer_load_dwordx4 %0, %4, %5, %6 offen sc1\n\tbuffer_load_dwordx4 %1, %4, %5, %6 offen offset:1024 sc1\n\t"
                   "buffer_load_dwordx4 %2, %4, %5, %7 offen sc1\n\tbuffer_load_dwordx4 %3, %4, %5, %7 offen offset:1024 sc1"
                   : "=&v"(hb[z][0]), "=&v"(hb[z][1]), "=&v"(hb[z][2]), "=&v"(hb[z][3]) : "v"(voffH), "s"(srdH), "s"(s0), "s"(s1) : "memory");
    };
#pragma unroll
    for (int z = 0; z <= hmax(-1); ++z) issueH(z);
    f32x16 acc[2][2] = {};
#pragma unroll
    for (int z = 0; z < 18; ++z) {
      if (z == ZO) lds_wait(lb + C_O + 4 * w, 1u);
      asm volatile("s_waitcnt vmcnt(%4)" : "+v"(hb[z][0]), "+v"(hb[z][1]), "+v"(hb[z][2]), "+v"(hb[z][3]) : "i"(p2_wait(z)) : "memory");
      if (z + 3 < 18) readA(z + 3);
      const int h0 = hmax(z - 1) + 1 <= hmax(z) ? hmax(z - 1) + 1 : -1, h1 = hmax(z - 1) + 2 <= hmax(z) ? hmax(z - 1) + 2 : -1;
      u32 s0 = 0, s1 = 0, gs = 0, gl = 0;
      if (h0 >= 0) { s0 = hb0 + hoff(h0); s1 = hb1 + hoff(h0); }
      SBAR();
      MM(acc[0][0], A[z][0], hb[z][0]); if (h0 >= 0) ldh<0>(hb[h0][0], srdH, voffH, s0);
      MM(acc[0][1], A[z][0], hb[z][2]); if (h0 >= 0) ldh<1024>(hb[h0][1], srdH, voffH, s0);
      MM(acc[1][0], A[z][2], hb[z][0]); if (h0 >= 0) ldh<0>(hb[h0][2], srdH, voffH, s1);
      MM(acc[1][1], A[z][2], hb[z][2]); if (h0 >= 0) ldh<1024>(hb[h0][3], srdH, voffH, s1);
      const int rf = refill(z);
      if (z < 10) WAITA(z + 3);
      if (rf >= 0) { gs = rf < 14 ? blk2(rf) * (32 * 896) : (4 * w + rf - 14) * (28 * 896); gl = lb + whome(rf) + w * 4096; }
      if (h1 >= 0) { s0 = hb0 + hoff(h1); s1 = hb1 + hoff(h1); }
      SBAR();
      MM(acc[0][0], A[z][1], hb[z][1]); if (rf >= 0) dmap<0>(rf < 14 ? srdD : srdO, vw0, gs, gl); if (h1 >= 0) ldh<0>(hb[h1][0], srdH, voffH, s0);
      MM(acc[0][1], A[z][1], hb[z][3]); if (rf >= 0) dmap<1024>(rf < 14 ? srdD : srdO, vw0 + 6144, gs, gl); if (h1 >= 0) ldh<1024>(hb[h1][1], srdH, voffH, s0);
      MM(acc[1][0], A[z][3], hb[z][1]); if (rf >= 0) dmap<2048>(rf < 14 ? srdD : srdO, vw0 + 2 * 6144, gs, gl); if (h1 >= 0) ldh<0>(hb[h1][2], srdH, voffH, s1);
      MM(acc[1][1], A[z][3], hb[z][3]); if (rf >= 0) dmap<3072>(rf < 14 ? srdD : srdO, vw0 + 3 * 6144, gs, gl); if (h1 >= 0) ldh<1024>(hb[h1][3], srdH, voffH, s1);
      SBAR();
    }

    int tz = __builtin_amdgcn_mbcnt_hi(~0u, __builtin_amdgcn_mbcnt_lo(~0u, 0)); PIN(tz);
    const size_t orow2 = (size_t)(ti * 64 + (w >> 1) * 32 + (tz & 31)) * D_ + ct * 64 + (w & 1) * 32 + ((tz >> 2) & 8);
    const u32 pb = lb + tz * 16;
#pragma unroll
    for (int k = 1; k < 4; ++k) {
      const f32x16& a = acc[k & 1][k >> 1];
#pragma unroll
      for (int g = 0; g < 4; ++g) *(LAS f32x4*)(pb + w * 4096 + (k - 1) * 16384 + g * 1024) = f32x4{a[4 * g], a[4 * g + 1], a[4 * g + 2], a[4 * g + 3]};
    }
    f32x16 mine = acc[0][0];
#pragma unroll
    for (int g = 0; g < 4; ++g) { mine[4 * g] += lo16(xr[g][0]); mine[4 * g + 1] += hi16(xr[g][0]); mine[4 * g + 2] += lo16(xr[g][1]); mine[4 * g + 3] += hi16(xr[g][1]); }
    PIN(mine);
    BAR();
#pragma unroll
    for (int g = 0; g < 4; ++g) {
#pragma unroll
      for (int k = 1; k < 4; ++k) {
        const f32x4 v = *(LAS f32x4*)(pb + (w ^ k) * 4096 + (k - 1) * 16384 + g * 1024);
#pragma unroll
        for (int e = 0; e < 4; ++e) mine[4 * g + e] += v[e];
      }
      if (g & 1) {
        const auto a = __builtin_amdgcn_permlane32_swap(pk2(mine[4 * g - 4], mine[4 * g - 3]), pk2(mine[4 * g], mine[4 * g + 1]), false, false);
        const auto b = __builtin_amdgcn_permlane32_swap(pk2(mine[4 * g - 2], mine[4 * g - 1]), pk2(mine[4 * g + 2], mine[4 * g + 3]), false, false);
        *(u32x4*)(out + orow2 + 8 * (g - 1)) = u32x4{a[0], b[0], a[1], b[1]};
      }
    }
    if (BID == 0 && w == 0 && tz == 0) __hip_atomic_store((GAS u32*)(ws + WS_EP), tag, RLX_AGENT);

    return;
    }
    tagc = tag;
  } else {

  int lq = l; PIN(lq);
  const int blk = (lq >> 4) & 1, q = (lq & 15) >> 2, p = lq & 3, h = lq >> 5, r = lq & 31;
  const u32 rowpart = (8 * h + q) * 128, rx = 64 * ((q >> 1) & 1);
  const auto rsw = __builtin_amdgcn_make_buffer_rsrc((void*)ws, 0, WS_END, 0x00020000);
  auto uaddr = [&](int n, int u) {
    u32 sub, cb;
    if (!ytype) { sub = 2 * n + blk; cb = (32 * u + 8 * p) ^ (32 * blk); }
    else if (u < 2) { sub = blk; cb = (32 * (2 * n + u) + 8 * p) ^ (32 * blk); }
    else if (u == 2) { sub = 2; cb = 64 * blk + ((32 * n + 8 * p) ^ (32 * blk)); }
    else { sub = 3; cb = ((qp == 7 ? 16 : 0) + 56 * n + 32 * blk + 8 * p) & 127; }
    return lb + sub * 4096 + rowpart + (cb ^ rx);
  };
  const u32 k_addr = lb + 16384 + rowpart + (((koff + 32 * blk + 8 * p) & 63) ^ rx);
  const int redl = (xcd & 2) ? 2 : 4;
  const bool reducer = (idx >> 2) == redl;
  const int ti = xcd * 4 + (idx & 3);
  const int m = wid >> 1, n = wid & 1;
  const int t0 = 2 * m + n, t1 = 2 * m + 1 - n;
  u32 aw[4];
#pragma unroll
  for (int u = 0; u < 4; ++u) aw[u] = uaddr(n, u);
  u32 ak = k_addr, av = k_addr ^ 64;
  u32 ax0 = lb + L_X + (32 * t0 + r) * 128 + 16 * (h ^ ((r >> 1) & 7));
  u32 ax1 = ax0 ^ 4096;
  f32x16 acc[4][2] = {}, ck = {}, cv = {};
  u32x4 X0 = {}, X1 = {}, W0 = {}, W1 = {}, W2 = {}, W3 = {}, WK = {}, WV = {}, X0n, X1n;
#define MA(u, Wr) do { MM(acc[u][0], Wr, X0); MM(acc[u][1], Wr, X1); } while (0)
#define TAIL(no) do { MA(3, W3); RW(W3, aw[3], no); SBAR(); MM(ck, WK, X0); RW(WK, ak, no); SBAR(); \
    MM(cv, X0, WV); RW(WV, av, no); SBAR(); X0 = X0n; X1 = X1n; } while (0)
#define PARTB(no) do { RW(W0, aw[0], no); X0n = ld128(ax0); X1n = ld128(ax1); SBAR(); \
    MM(acc[2][1], W2, X1); RW(W1, aw[1], no); RW(W2, aw[2], no); SBAR(); TAIL(no); } while (0)
#define FULL(no) do { MA(0, W0); RW(W0, aw[0], no); X0n = ld128(ax0); SBAR(); MA(1, W1); RW(W1, aw[1], no); X1n = ld128(ax1); SBAR(); \
    MA(2, W2); RW(W2, aw[2], no); SBAR(); TAIL(no); } while (0)
#define XSTEP(xx) do { ax0 ^= (xx); ax1 ^= (xx); PIN(ax0); PIN(ax1); } while (0)
#pragma unroll
  for (int u = 0; u < 4; ++u) { PIN(acc[u][0]); PIN(acc[u][1]); }
  PIN(ck); PIN(cv); PIN(X0); PIN(X1); PIN(W2); PIN(W3); PIN(WK); PIN(WV);
  SBAR();
  if (wid == 0) {
    const u32 tag0 = __builtin_amdgcn_readfirstlane(ep) * 1664525u + 1013904223u;
    *(LAS u32*)(lb + L_CTL + 4 * l) = l == 0 ? tag0 : 0u;
    float sx = 0.f, sq = 0.f;
#pragma unroll
    for (int i = 0; i < 7; ++i)
#pragma unroll
      for (int e = 0; e < 4; ++e) { sx = dot2(xs[i][e], 0x3f803f80u, sx); sq = dot2(xs[i][e], xs[i][e], sq); }
#pragma unroll
    for (int o = 1; o < 8; o <<= 1) { sx += U2F(__builtin_amdgcn_ds_bpermute((l ^ o) * 4, F2U(sx))); sq += U2F(__builtin_amdgcn_ds_bpermute((l ^ o) * 4, F2U(sq))); }
    const float mean = sx * (1.f / D_);
    const float var = __builtin_fmaxf(sq * (1.f / D_) - mean * mean, 0.f);
    const float sd = __builtin_amdgcn_sqrtf(rbf(rbf(var) + 1e-5f));
    if ((l & 7) == 0) __hip_atomic_store((GAS u64*)(ws + WS_ST + (panel * 128 + j * 8 + (l >> 3)) * 8), (u64)pk2(mean, sd) | ((u64)tag0 << 32), RLX_AGENT);
  }
  WAITV(1);
  if (wid & 1) WAITV(0);
  BAR();

  u32 delta = L_SLOT;
#pragma unroll 1
  for (int it = 0; it < 6; ++it) {
    PARTB(0u);
    XSTEP(32u); FULL(2048u);
    XSTEP(96u); MA(0, W0); MA(1, W1); BAR(); MM(acc[2][0], W2, X0); PARTB(L_WH);
    XSTEP(32u); FULL(L_WH + 2048u);
    MM(acc[0][0], W0, X0);
    ax0 = (ax0 ^ 96u) + delta; ax1 = (ax1 ^ 96u) + delta; aw[0] += delta; PIN(ax0); PIN(ax1); PIN(aw[0]); SBAR();
    MM(acc[0][1], W0, X1);
    aw[1] += delta; aw[2] += delta; aw[3] += delta; PIN(aw[1]); PIN(aw[2]); PIN(aw[3]); SBAR();
    MM(acc[1][0], W1, X0);
    ak += delta; av += delta; PIN(ak); PIN(av); delta = 0u - delta;
    SBAR();
    MM(acc[1][1], W1, X1);
    BAR();
    MM(acc[2][0], W2, X0);
  }
  const u32 wpl = (4 * h + q) * 56 + 32 * blk + 8 * p;
  auto prephi = [&](const f32x16& a, u32 wp, float& mx, float& mn) {
    u32x4 b0, b1;
#pragma unroll
    for (int e = 0; e < 4; ++e) { b0[e] = pk2(a[2 * e], a[2 * e + 1]); b1[e] = pk2(a[8 + 2 * e], a[9 + 2 * e]); }
    b1[2] = h ? 0u : b1[2]; b1[3] = h ? 0u : b1[3];
    f32x16 pz = {};
    pz = mfma(ldtr2(wp, wp + 8 * 56), b0, pz);
    pz = mfma(ldtr2(wp + 16 * 56, wp + 24 * 56), b1, pz);
    float x1 = __builtin_fmaxf(pz[0], pz[1]), n1 = __builtin_fminf(pz[0], pz[1]), x2 = __builtin_fmaxf(pz[12], pz[13]), n2 = __builtin_fminf(pz[12], pz[13]);
#pragma unroll
    for (int e = 2; e < 12; e += 2) { x1 = max3(x1, pz[e], pz[e + 1]); n1 = min3(n1, pz[e], pz[e + 1]); }
    x2 = max3(x2, pz[14], pz[15]); n2 = min3(n2, pz[14], pz[15]);
    float lo, hi, lo2, hi2;
    xhalf(x1, lo, hi); xhalf(x2, lo2, hi2); mx = max3(lo, hi, lo2);
    xhalf(n1, lo, hi); xhalf(n2, lo2, hi2); mn = min3(lo, hi, lo2);
    return pz;
  };
  const u32 fx = lb + L_FIX + l * 16;
  const u32 nm0 = *(LAS u32*)(lb + L_ST + 512 + (32 * t0 + r) * 4), nm1 = *(LAS u32*)(lb + L_ST + 512 + (32 * t1 + r) * 4);
  const float rs0 = *(LAS float*)(lb + L_ST + (32 * t0 + r) * 4), rs1 = *(LAS float*)(lb + L_ST + (32 * t1 + r) * 4);
  const u32 tag = __builtin_amdgcn_readfirstlane(*(LAS u32*)(lb + L_CTL));
  const u32x4 M0 = {nm0, nm0, nm0, nm0}, M1 = {nm1, nm1, nm1, nm1};
  PARTB(0u);
  {
    u32x4 K1, V1, Y1;
    RW(K1, ak, 2048u); RW(V1, av, 2048u); Y1 = ld128(ax0 ^ 32u); SBAR();
    MM(ck, WK, X0); MM(cv, X0, WV);
    RW(WK, ak, L_WH); RW(WV, av, L_WH); X0n = ld128(ax0 ^ 64u); SBAR();
    MA(0, W0); RW(W0, aw[0], 2048u); SBAR();
    MM(ck, K1, Y1); MM(cv, Y1, V1);
    RW(K1, ak, L_WH + 2048u); RW(V1, av, L_WH + 2048u); X1n = ld128(ax0 ^ 96u); SBAR();
    MA(1, W1); RW(W1, aw[1], 2048u); SBAR();
    MM(ck, WK, X0n); MM(cv, X0n, WV);
    WK = ld128(lb + L_FIXK + l * 16); WV = ld128(lb + L_FIXK + 1024 + l * 16); SBAR();
    MA(2, W2); RW(W2, aw[2], 2048u); SBAR();
    MM(ck, K1, X1n); MM(cv, X1n, V1);
    X1n = ld128(ax1 ^ 32u); SBAR();
    MM(ck, WK, M0); MM(cv, M0, WV);
    const u32 wp = lb + L_WPK + wpl;
    K1 = ldtr2(wp, wp + 8 * 56); V1 = ldtr2(wp + 16 * 56, wp + 24 * 56); SBAR();
    MM(acc[3][0], W3, X0);
    u32x2 sd[4];
#pragma unroll
    for (int g = 0; g < 4; ++g) sd[g] = *(LAS u32x2*)(lb + L_SD + (32 * t0 + 8 * g + 4 * h) * 2);
    SBAR();
    MM(acc[3][1], W3, X1);
    RW(W3, aw[3], 2048u); X0 = Y1; X1 = X1n;
    u32x4 b0, b1, vb0, vb1, o0, o1;
#define GRP(...) do { __VA_ARGS__; SBAR(); } while (0)
    GRP(b0 = u32x4{pk2(ck[0], ck[1]), pk2(ck[2], ck[3]), pk2(ck[4], ck[5]), pk2(ck[6], ck[7])}; PIN(b0));
    MM(acc[0][0], W0, X0);
    GRP(b1 = u32x4{pk2(ck[8], ck[9]), pk2(ck[10], ck[11]), h ? 0u : pk2(ck[12], ck[13]), h ? 0u : pk2(ck[14], ck[15])}; PIN(b1));
    f32x16 pz = {};
    MM(pz, K1, b0);
    GRP(vb0 = u32x4{pk2(cv[0], cv[1]), pk2(cv[2], cv[3]), pk2(cv[4], cv[5]), pk2(cv[6], cv[7])}; PIN(vb0));
    MM(pz, V1, b1);
    GRP(vb1 = u32x4{pk2(cv[8], cv[9]), pk2(cv[10], cv[11]), pk2(cv[12], cv[13]), pk2(cv[14], cv[15])}; PIN(vb1));
    MM(acc[0][1], W0, X1);
    GRP(vb0 = r == 28 ? u32x4{sd[0][0], sd[0][1], sd[1][0], sd[1][1]} : vb0; st128(lb + L_VF + t0 * 2048 + l * 16, vb0));
    MM(acc[1][0], W1, X0);
    GRP(vb1 = r == 28 ? u32x4{sd[2][0], sd[2][1], sd[3][0], sd[3][1]} : vb1; st128(lb + L_VF + t0 * 2048 + 1024 + l * 16, vb1));
    MM(acc[1][1], W1, X1);
    BAR();
    float x1, n1, x2, n2, lo, hi, lo2, hi2;
    GRP(RW(W0, aw[0], L_WH); X0n = ld128(ax0 ^ 64u));
    MM(acc[2][0], W2, X0);
    GRP(X1n = ld128(ax1 ^ 64u); RW(W1, aw[1], L_WH); x1 = max3(pz[0], pz[1], pz[2]); n1 = min3(pz[0], pz[1], pz[2]); PIN(x1); PIN(n1));
    MM(acc[2][1], W2, X1);
    GRP(RW(W2, aw[2], L_WH); x2 = max3(pz[12], pz[13], pz[14]); n2 = min3(pz[12], pz[13], pz[14]); x1 = max3(x1, pz[3], pz[4]); n1 = min3(n1, pz[3], pz[4]); PIN(x1); PIN(n1); PIN(x2); PIN(n2));
    MM(acc[3][0], W3, X0);
    GRP(x2 = max3(x2, pz[15], pz[15]); n2 = min3(n2, pz[15], pz[15]); x1 = max3(x1, pz[5], pz[6]); n1 = min3(n1, pz[5], pz[6]); x1 = max3(x1, pz[7], pz[8]); n1 = min3(n1, pz[7], pz[8]); PIN(x1); PIN(n1); PIN(x2); PIN(n2));
    MM(acc[3][1], W3, X1);
    GRP(RW(W3, aw[3], L_WH); X0 = X0n; X1 = X1n; x1 = max3(x1, pz[9], pz[10]); n1 = min3(n1, pz[9], pz[10]); x2 = h ? pz[11] : x2; n2 = h ? pz[11] : n2; PIN(x1); PIN(n1); PIN(x2); PIN(n2));
    MM(acc[0][0], W0, X0);
    GRP(x1 = max3(x1, x2, pz[11]); n1 = min3(n1, n2, pz[11]); o0 = u32x4{pk2(pz[0], pz[1]), pk2(pz[2], pz[3]), pk2(pz[4], pz[5]), pk2(pz[6], pz[7])}; PIN(x1); PIN(n1));
    MM(acc[0][1], W0, X1);
    GRP(RW(W0, aw[0], L_WH + 2048u); X0n = ld128(ax0 ^ 96u); st128(lb + L_PK + t0 * 2048 + l * 16, o0); xhalf(x1, lo, hi); PIN(lo); PIN(hi));
    MM(acc[1][0], W1, X0);
    GRP(xhalf(n1, lo2, hi2); x1 = max3(lo, hi, hi); n1 = min3(lo2, hi2, hi2); PIN(x1); PIN(n1));
    MM(acc[1][1], W1, X1);
    GRP(RW(W1, aw[1], L_WH + 2048u); X1n = ld128(ax1 ^ 96u);
        o1 = u32x4{pk2(pz[8], pz[9]), pk2(pz[10], pz[11]), pk2(h ? x1 : pz[12], h ? n1 : pz[13]), pk2(pz[14], pz[15])}; st128(lb + L_PK + t0 * 2048 + 1024 + l * 16, o1));
    MA(2, W2);
    asm volatile("s_waitcnt lgkmcnt(0)" ::: "memory");
    lds_sig(lb + C_PK + 4 * t0);

    GRP(RW(W2, aw[2], L_WH + 2048u); WK = ld128(fx + n * 4096));
    MA(3, W3);
    GRP(RW(W3, aw[3], L_WH + 2048u); WV = ld128(fx + n * 4096 + 1024); X0 = X0n; X1 = X1n);
    MA(0, W0);
    GRP(K1 = ld128(fx + n * 4096 + 2048); V1 = ld128(fx + n * 4096 + 3072));
    MM(acc[0][0], WK, M0); MM(acc[0][1], WK, M1);
    MA(1, W1);
    acc[1][0] = mfma(WV, M0, acc[1][0]); acc[1][1] = mfma(WV, M1, acc[1][1]);
    acc[2][0] = mfma(W2, X0, acc[2][0]); acc[2][1] = mfma(W2, X1, acc[2][1]); acc[2][0] = mfma(K1, M0, acc[2][0]); acc[2][1] = mfma(K1, M1, acc[2][1]);
    acc[3][0] = mfma(W3, X0, acc[3][0]); acc[3][1] = mfma(W3, X1, acc[3][1]); acc[3][0] = mfma(V1, M0, acc[3][0]); acc[3][1] = mfma(V1, M1, acc[3][1]);
  }
  auto ffn = [&](const f32x16& a, float rs, int tile, int unit) {
    const float r2 = rs * rs;
    u32x4 o;
#pragma unroll
    for (int e = 0; e < 4; ++e) o[e] = pk2(__builtin_fmaxf(a[2 * e] * r2, 0.f) * a[8 + 2 * e], __builtin_fmaxf(a[2 * e + 1] * r2, 0.f) * a[9 + 2 * e]);
    st_wt(rsw, l * 16 + WS_H + ((panel * 4 + tile) * 144 + unit) * 1024, o);
  };
  const int ub = ytype ? line0 * 4 + 2 * n : (line0 + n) * 4;
  ffn(acc[0][0], rs0, t0, ub); ffn(acc[0][1], rs1, t1, ub);
  ffn(acc[1][0], rs0, t0, ub + 1); ffn(acc[1][1], rs1, t1, ub + 1);
#pragma unroll
  for (int g = 0; g < 10; ++g) { __builtin_amdgcn_sched_group_barrier(0x8, 1, 0); __builtin_amdgcn_sched_group_barrier(0x2, 6, 0); }
  PIN(acc[2][0]); PIN(acc[2][1]); PIN(acc[3][0]); PIN(acc[3][1]);
  const int u2 = ytype ? lineh * 4 + 2 * sec + n : ub + 2;
  ffn(acc[2][0], rs0, t0, u2); ffn(acc[2][1], rs1, t1, u2);
  if (!ytype) {
    ffn(acc[3][0], rs0, t0, ub + 3); ffn(acc[3][1], rs1, t1, ub + 3);
  } else {
    lds_sig(lb + C_S + 4 * wid);
    auto qout = [&](const f32x16& a, float rs, int tile, bool first) {
      float mx, mn, lo, hi;
      const f32x16 pz = prephi(a, lb + L_WPQ + n * 1568 + wpl, mx, mn);
      const float sc = rs * LOG2E;
      mx *= -sc; mn *= sc;
      const float mx2 = h ? -__builtin_inff() : mx, mn2 = h ? -__builtin_inff() : mn;
      float ep[16], en[16], s0 = 0.f, s1 = 0.f, s2 = 0.f, s3 = 0.f;
#pragma unroll
      for (int e = 0; e < 16; ++e) {
        ep[e] = ex2(pz[e] * sc + (e < 12 ? mx : mx2)); en[e] = ex2((e < 12 ? mn : mn2) - pz[e] * sc);
        if (e & 1) { s1 += ep[e]; s3 += en[e]; } else { s0 += ep[e]; s2 += en[e]; }
        if (first && e == 5) {
          PIN(s0); PIN(s1); PIN(s2); PIN(s3); SBAR();
          WAITV(0);
          if (lds_inc(lb + C_Y) == 3u && l == 0) __hip_atomic_store((GAS u32*)(ws + WS_FH + (panel * 16 + j) * 128), tag, RLX_AGENT);
        }
      }
      xhalf(s0 + s1, lo, hi); const float ip = __builtin_amdgcn_rcpf(lo + hi);
      xhalf(s2 + s3, lo, hi); const float in = __builtin_amdgcn_rcpf(lo + hi);
      const u32 so = l * 16 + WS_PQ + ((panel * 4 + tile) * 16 + 2 * qp + n) * 4096;
#pragma unroll
      for (int s = 0; s < 2; ++s) {
        u32x4 o0, o1;
#pragma unroll
        for (int e = 0; e < 4; ++e) { o0[e] = pk2(ep[8 * s + 2 * e] * ip, ep[8 * s + 2 * e + 1] * ip); o1[e] = pk2(en[8 * s + 2 * e] * in, en[8 * s + 2 * e + 1] * in); }
        st_wt(rsw, so + s * 1024, o0); st_wt(rsw, so + 2048 + s * 1024, o1);
      }
      SBAR();
    };
    qout(acc[3][0], rs0, t0, true); qout(acc[3][1], rs1, t1, false);
  }
  lds_sig(lb + C_S + 4 * wid);

  WAITV(0);
  if (lds_inc(lb + C_A) == 3u && l == 0) __hip_atomic_store((GAS u32*)(ws + (ytype ? WS_FQ + (panel * 8 + qp) * 128 : WS_FH + (panel * 16 + j) * 128)), tag, RLX_AGENT);

  if (!reducer) {
#pragma unroll 1
    for (int i = 0; i < (ytype ? 6 : 8); ++i) __builtin_amdgcn_s_sleep(16);
    wait_flags(ws + WS_FO + ((ti >> 1) * 16 + 4 * w + (l & 3)) * 128, tag);
    lds_sig(lb + C_O + 4 * w);

    return;
  }
  tagc = tag;
  }

  {
    const u32 tag = tagc;
    int lq = l; PIN(lq);
    const int h = lq >> 5;
    const int ti = xcd * 4 + (idx & 3), head = ti >> 1, pnl = 8 * (ti & 1) + wid, hf = 2 * head + (wid >> 2);
    const auto rsw = __builtin_amdgcn_make_buffer_rsrc((void*)ws, 0, WS_END, 0x00020000);
    const u32x4 srdS = mksrd(ws, WS_END);
    u32x4 v[8], pf[16];
#pragma unroll
    for (int i = 0; i < 8; ++i) asm volatile("" : "=v"(v[i]), "=v"(pf[2 * i]), "=v"(pf[2 * i + 1]));
    u32 done = 0;
    bool sent = false;
    const u32 fko = l < 8 ? WS_FKV + (hf * 16 + 8 * (wid & 1) + l) * 128 : WS_FQ + (pnl * 8 + (head >> 1)) * 128;
    lds_wait4(lb + C_S);

#pragma unroll 1
    for (int spin = 0; spin < SPIN_CAP && !(sent && done == 0x1ffu); ++spin) {
      u32 fv = 0;
      if (l < 9 && !(done >> l & 1)) fv = __hip_atomic_load((GAS u32*)(ws + fko), RLX_AGENT);
      const u32 now = (u32)__builtin_amdgcn_ballot_w64(fv == tag) & 0x1ffu & ~done;
#pragma unroll
      for (int i = 0; i < 8; ++i)
        if (now >> i & 1) asm volatile("buffer_load_dwordx4 %0, %1, %2, %3 offen sc1" : "+v"(v[i]) : "v"(l * 16), "s"(srdS), "s"(WS_KVP + (hf * 16 + 8 * (wid & 1) + i) * 2048 + ((wid >> 1) & 1) * 1024) : "memory");
      done |= now;
      if (!sent && (done & 0xffu) == 0xffu) {
        asm volatile("s_waitcnt vmcnt(0)" : "+v"(v[0]), "+v"(v[1]), "+v"(v[2]), "+v"(v[3]), "+v"(v[4]), "+v"(v[5]), "+v"(v[6]), "+v"(v[7])::"memory");
        f32x4 sl = {}, sh = {};
#pragma unroll
        for (int i = 0; i < 8; ++i)
#pragma unroll
          for (int e = 0; e < 4; ++e) { sl[e] += lo16(v[i][e]); sh[e] += hi16(v[i][e]); }
        *(LAS f32x4*)(lb + wid * 2048 + l * 16) = sl; *(LAS f32x4*)(lb + wid * 2048 + 1024 + l * 16) = sh;
        lds_sig(lb + C_K2 + 4 * wid);
        sent = true;

      }
#pragma unroll
      for (int u = 0; u < 4; ++u)
        if (now >> 8) {
          asm volatile("buffer_load_dwordx4 %0, %4, %5, %6 offen sc1\n\tbuffer_load_dwordx4 %1, %4, %5, %6 offen offset:1024 sc1\n\t"
                       "buffer_load_dwordx4 %2, %4, %5, %6 offen offset:2048 sc1\n\tbuffer_load_dwordx4 %3, %4, %5, %6 offen offset:3072 sc1"
                       : "+v"(pf[4 * u]), "+v"(pf[4 * u + 1]), "+v"(pf[4 * u + 2]), "+v"(pf[4 * u + 3])
                       : "v"(l * 16), "s"(srdS), "s"(WS_PQ + ((pnl * 4 + u) * 16 + head) * 4096) : "memory");
      }
      __builtin_amdgcn_s_sleep(2);
    }
    lds_wait4(lb + C_K2); lds_wait4(lb + C_K2 + 16);
    u32x4 kv[4];
#pragma unroll
    for (int s = 0; s < 4; ++s) {
      const u32 a = lb + s * 4096 + l * 16;
      const f32x4 lo = *(LAS f32x4*)(a) + *(LAS f32x4*)(a + 2048), hi = *(LAS f32x4*)(a + 1024) + *(LAS f32x4*)(a + 3072);
      kv[s] = u32x4{pk2(lo[0], hi[0]), pk2(lo[1], hi[1]), pk2(lo[2], hi[2]), pk2(lo[3], hi[3])};
    }
    WAITV(0);
#pragma unroll
    for (int i = 0; i < 16; ++i) PIN(pf[i]);

    f32x16 oc[4] = {};
#pragma unroll
    for (int u = 0; u < 4; ++u)
#pragma unroll
      for (int s = 0; s < 4; ++s) oc[u] = mfma(kv[s], pf[4 * u + s], oc[u]);
#pragma unroll
    for (int u = 0; u < 4; ++u) {
      const float rd = xhalf_hi(__builtin_amdgcn_rcpf(oc[u][12] + 1e-6f));
      u32x4 o0, o1;
#pragma unroll
      for (int e = 0; e < 4; ++e) { o0[e] = pk2(oc[u][2 * e] * rd, oc[u][2 * e + 1] * rd); o1[e] = pk2(oc[u][8 + 2 * e] * rd, oc[u][9 + 2 * e] * rd); }
      o1[2] = h ? 0u : o1[2]; o1[3] = h ? 0u : o1[3];
      const u32 so = l * 16 + WS_H + ((pnl * 4 + u) * 144 + 112 + 2 * head) * 1024;
      st_wt(rsw, so, o0); st_wt(rsw, so + 1024, o1);
    }

    WAITV(0);
    if (l == 0) __hip_atomic_store((GAS u32*)(ws + WS_FO + (pnl * 16 + head) * 128), tag, RLX_AGENT);

  }
}

#if defined(__HIP_DEVICE_COMPILE__)
#pragma clang attribute pop
#endif

extern "C" void kernel_launch(void* const* d_in, const int* in_sizes, int n_in, void* d_out, int out_size, void* d_ws, size_t ws_size,
                              hipStream_t stream) {
  fused<<<256, 512, 0, stream>>>((const u16*)d_in[0], (const u16*)d_in[1], (const u16*)d_in[2], (const u16*)d_in[3], (const u16*)d_in[5],
                                 (const u16*)d_in[6], (const u16*)d_in[7], (char*)d_ws, (u16*)d_out);
}
```
